```python
import torch

def _softmax(x, axis=-1):
    return torch.softmax(x, dim=axis)

def _gelu(x):
    return torch.nn.functional.gelu(x, approximate='tanh')

def _silu(x):
    return torch.nn.functional.silu(x)

def _sigmoid(x):
    return torch.sigmoid(x)

def _layernorm(x, axis=-1, eps=1e-05):
    m = x.mean(dim=axis, keepdim=True)
    v = x.var(dim=axis, keepdim=True, unbiased=False)
    return (x - m) / torch.sqrt(v + eps)

def _scatter_add(tgt, idx, src, axis=0):
    return tgt.clone().index_add_(axis, idx, src)

def _segment_sum(src, seg, n):
    out = torch.zeros((n,) + src.shape[1:], dtype=src.dtype)
    return out.index_add_(0, seg, src)

def _pad(x, pw):
    flat = tuple((v for pair in reversed(pw) for v in pair))
    return torch.nn.functional.pad(x, flat)
kernel_name = 'adaLN_cond__ssm_conv_s4__glu_gated_bilinear__prompt'

def setup_inputs():

    _w = lambda a: (lambda t: t.to(torch.bfloat16) if t.is_floating_point() else t)(torch.as_tensor(a))

    def _A_setup():
        B, L, D, N = (1, 2048, 448, 256)
        A_re = -(lambda _x: torch.exp(torch.as_tensor(_x)))((lambda *s: torch.randn(*s, dtype=torch.bfloat16) * 3.2)(D, N))
        A_im = torch.pi * (lambda *s: torch.randn(*s, dtype=torch.bfloat16) * 0.1)(D, N)
        B_re = (lambda *s: torch.randn(*s, dtype=torch.bfloat16) * 0.1)(D, N)
        B_im = (lambda *s: torch.randn(*s, dtype=torch.bfloat16) * 0.1)(D, N)
        C_re = (lambda *s: torch.randn(*s, dtype=torch.bfloat16) * 0.1)(D, N)
        C_im = (lambda *s: torch.randn(*s, dtype=torch.bfloat16) * 0.1)(D, N)
        log_dt = (lambda *s: torch.randn(*s, dtype=torch.bfloat16) * 0.1)(D) * 0.5 - 4.0
        Dskip = (lambda *s: torch.randn(*s, dtype=torch.bfloat16) * 0.1)(D)
        return {'u': _w((lambda *s: torch.randn(*s, dtype=torch.bfloat16) * 0.05)(B, L, D)), 'A_re': _w(A_re), 'A_im': _w(A_im), 'B_re': _w(B_re), 'B_im': _w(B_im), 'C_re': _w(C_re), 'C_im': _w(C_im), 'log_dt': _w(log_dt), 'Dskip': _w(Dskip)}
    _A_in = _A_setup()

    def _B_setup():
        B, S, D, F = (1, 2048, 448, 1792)
        return {'x': _w((lambda *s: torch.randn(*s, dtype=torch.bfloat16) * 0.05)(B, S, D)), 'w_gate': _w((lambda *s: torch.randn(*s, dtype=torch.bfloat16) * 1.6)(D, F) * D ** (-0.5)), 'w_up': _w((lambda *s: torch.randn(*s, dtype=torch.bfloat16) * 1.6)(D, F) * D ** (-0.5)), 'w_down': _w((lambda *s: torch.randn(*s, dtype=torch.bfloat16) * 1.6)(F, D) * F ** (-0.5))}
    _B_in = _B_setup()
    return {'A_u': _A_in['u'], 'A_A_re': _A_in['A_re'], 'A_A_im': _A_in['A_im'], 'A_B_re': _A_in['B_re'], 'A_B_im': _A_in['B_im'], 'A_C_re': _A_in['C_re'], 'A_C_im': _A_in['C_im'], 'A_log_dt': _A_in['log_dt'], 'A_Dskip': _A_in['Dskip'], 'B_x': _B_in['x'], 'B_w_gate': _B_in['w_gate'], 'B_w_up': _B_in['w_up'], 'B_w_down': _B_in['w_down'], 'c': _w((lambda *s: torch.randn(*s, dtype=torch.bfloat16) * 0.2)(1, 448) * 8.0), 'w_mod': _w((lambda *s: torch.randn(*s, dtype=torch.bfloat16) * 0.2)(448, 6 * 448) * 448 ** (-0.5) * 8.0)}

def reference(A_u, A_A_re, A_A_im, A_B_re, A_B_im, A_C_re, A_C_im, A_log_dt, A_Dskip, B_x, B_w_gate, B_w_up, B_w_down, c, w_mod):

    def _A_ref(u, A_re, A_im, B_re, B_im, C_re, C_im, log_dt, Dskip):
        Bsz, L, D = u.shape
        dt = torch.exp(log_dt)[:, None]
        A = A_re + 1j * A_im
        Bc = B_re + 1j * B_im
        Cc = C_re + 1j * C_im
        logdA = dt * A
        dB = dt * Bc
        l = (lambda x, dt: x.to(dt))(torch.arange(L), A_re.dtype)
        P = torch.exp(logdA[:, :, None] * l[None, None, :])
        K = torch.einsum('dn,dnl,dn->dl', Cc, P, dB).real
        L2 = 2 * L
        Kp = _pad(K, ((0, 0), (0, L)))
        up = (lambda x, *d: x.permute(*d))(u, 0, 2, 1)
        up = _pad(up, ((0, 0), (0, 0), (0, L)))
        Kf = (lambda x, axes=(-2, -1): torch.fft.rfft2(x.float(), dim=axes))(Kp[:, None, :], axes=(-2, -1))
        Uf = (lambda x, axes=(-2, -1): torch.fft.rfft2(x.float(), dim=axes))(up[:, :, None, :], axes=(-2, -1))
        Yf = Uf * Kf[None, :, :, :]
        y = (lambda x, s, axes=(-2, -1): torch.fft.irfft2(x, s=s, dim=axes).to(torch.bfloat16))(Yf, (1, L2), axes=(-2, -1))[:, :, 0, :L]
        y = (lambda x, *d: x.permute(*d))(y, 0, 2, 1) + u * Dskip[None, None, :]
        return y

    def _B_ref(x, w_gate, w_up, w_down):
        g = torch.einsum('bsd,df->bsf', x, w_gate)
        u = torch.einsum('bsd,df->bsf', x, w_up)
        h = g * u
        return torch.einsum('bsf,fd->bsd', h, w_down)
    x = A_u
    mod = (_silu(c) @ w_mod).reshape(c.shape[0], 6, 1, -1)
    sa, ca, ga, sb, cb, gb = (mod[:, 0].clone(), mod[:, 1].clone(), mod[:, 2].clone(), mod[:, 3].clone(), mod[:, 4].clone(), mod[:, 5].clone())
    h = _layernorm(x, axis=-1) * (1 + ca) + sa
    x = x + ga * _A_ref(h, A_A_re, A_A_im, A_B_re, A_B_im, A_C_re, A_C_im, A_log_dt, A_Dskip)
    h = _layernorm(x, axis=-1) * (1 + cb) + sb
    x = x + gb * _B_ref(h, B_w_gate, B_w_up, B_w_down)
    return x
```

```cpp
#include <hip/hip_runtime.h>
#include <hip/hip_bf16.h>
#include <stdint.h>

typedef unsigned short u16;
typedef __attribute__((ext_vector_type(8))) __bf16 bf16x8;
typedef __attribute__((ext_vector_type(2))) __bf16 bf16x2;
typedef __attribute__((ext_vector_type(4))) short s16x4;
typedef __attribute__((ext_vector_type(4))) float f32x4;
typedef __attribute__((ext_vector_type(2))) float f32x2;
typedef __attribute__((ext_vector_type(16))) float f32x16;
typedef __attribute__((ext_vector_type(4))) unsigned u32x4;
typedef __attribute__((ext_vector_type(2))) unsigned u32x2;
#define LDS __attribute__((address_space(3)))
#define GLB __attribute__((address_space(1)))
#define DI __device__ __forceinline__

constexpr int L_ = 2048, D_ = 448, NM = 256, F_ = 1792;
constexpr float EPS = 1e-5f;

constexpr size_t WS_XT = 0;
constexpr size_t WS_X1T = WS_XT + (size_t)D_ * L_ * 2;
constexpr size_t WS_STATS = WS_X1T + (size_t)D_ * L_ * 2;
constexpr size_t WS_MOD = WS_STATS + L_ * 8;
constexpr int RG_STRIDE = 2080;
constexpr size_t WS_RG = WS_MOD + 2688 * 4 + 256;
constexpr size_t WS_WPF = WS_RG + (size_t)D_ * RG_STRIDE * 2;
constexpr size_t WS_UB = WS_WPF + (size_t)112 * 28 * 1024;
constexpr size_t WS_WDF = WS_UB + 2 * 2 * 3584 * 4;
constexpr size_t WS_HF = WS_WDF + (size_t)14 * 112 * 1024;
constexpr size_t WS_END = WS_HF + (size_t)64 * 112 * 1024;

DI float bf2f(u16 h) { return __uint_as_float((unsigned)h << 16); }
DI float bflo(unsigned u) { return __uint_as_float(u << 16); }
DI float bfhi(unsigned u) { return __uint_as_float(u & 0xffff0000u); }
DI unsigned pk2(float a, float b) { bf16x2 r = {(__bf16)a, (__bf16)b}; return __builtin_bit_cast(unsigned, r); }
DI float rbf(float a) { return (float)(__bf16)a; }

template <int CTRL, int RM>
DI float dpp_add(float v) {
    int t = __builtin_amdgcn_update_dpp(0, __float_as_int(v), CTRL, RM, 0xf, true);
    return v + __int_as_float(t);
}
DI float wave_sum(float v) {
    v = dpp_add<0x111, 0xf>(v);
    v = dpp_add<0x112, 0xf>(v);
    v = dpp_add<0x114, 0xf>(v);
    v = dpp_add<0x118, 0xf>(v);
    v = dpp_add<0x142, 0xa>(v);
    v = dpp_add<0x143, 0xc>(v);
    return __int_as_float(__builtin_amdgcn_readlane(__float_as_int(v), 63));
}
DI f32x4 mfma16(bf16x8 a, bf16x8 b, f32x4 c) { return __builtin_amdgcn_mfma_f32_16x16x32_bf16(a, b, c, 0, 0, 0); }
DI f32x16 mfma32(bf16x8 a, bf16x8 b, f32x16 c) { return __builtin_amdgcn_mfma_f32_32x32x16_bf16(a, b, c, 0, 0, 0); }
DI bf16x8 as_frag(u32x4 v) { return __builtin_bit_cast(bf16x8, v); }
DI bf16x8 tr_frag(LDS char* p0, LDS char* p1) {
    s16x4 a = __builtin_amdgcn_ds_read_tr16_b64_v4i16((LDS s16x4*)p0);
    s16x4 b = __builtin_amdgcn_ds_read_tr16_b64_v4i16((LDS s16x4*)p1);
    return __builtin_bit_cast(bf16x8, __builtin_shufflevector(a, b, 0, 1, 2, 3, 4, 5, 6, 7));
}

struct cf { float re, im; };
DI cf cmul(cf a, cf b) { cf r; r.re = a.re * b.re - a.im * b.im; r.im = a.re * b.im + a.im * b.re; return r; }
DI cf csq(cf a) { cf r; r.re = a.re * a.re - a.im * a.im; r.im = 2.f * a.re * a.im; return r; }
DI cf zpow(float a2, float br, float l) {
    float mag = __builtin_amdgcn_exp2f(a2 * l);
    float fr = __builtin_amdgcn_fractf(br * l);
    cf r; r.re = mag * __builtin_amdgcn_cosf(fr); r.im = mag * __builtin_amdgcn_sinf(fr);
    return r;
}

constexpr int K1_TROW = 1056;
constexpr int K1_TCH = 57 * K1_TROW;
constexpr int K1_OFF_KC = 2 * K1_TCH;
constexpr int K1_OFF_XS = K1_OFF_KC + 2 * 48 * 16 * 4;
constexpr int K1_OFF_MR = K1_OFF_XS + 8 * 448 * 2;
constexpr int K1_LDS = K1_OFF_MR + 8 * 8 * 4;

__global__ __launch_bounds__(512) void k1_prep(
    const u16* __restrict__ x, const u16* __restrict__ Are, const u16* __restrict__ Aim,
    const u16* __restrict__ Bre, const u16* __restrict__ Bim, const u16* __restrict__ Cre,
    const u16* __restrict__ Cim, const u16* __restrict__ logdt, const u16* __restrict__ cvec,
    const u16* __restrict__ wmod, u16* __restrict__ xT, float* __restrict__ stats,
    float* __restrict__ mod, u16* __restrict__ Rg)
{
    extern __shared__ __attribute__((aligned(16))) char smem[];
    LDS char* lds = (LDS char*)smem;
    const int tid = threadIdx.x, lane = tid & 63, wid = tid >> 6, b = blockIdx.x;

    const int xq = b >> 3, xx = b & 7;
    const int r0 = 64 * (xx * 4 + (xq >> 3)) + 8 * (xq & 7);
    u32x4 xv = {0, 0, 0, 0};
    if (lane < 56) xv = *(const u32x4*)(x + (size_t)(r0 + wid) * D_ + lane * 8);
    const bool doB = b >= 88;
    const int j0 = 16 * (b - 88) + 8 * (wid >> 2);
    const int kB = tid & 255;
    u32x4 w0 = {0, 0, 0, 0}, w1 = {0, 0, 0, 0};
    float c0 = 0.f, c1 = 0.f;
    if (doB) {
        w0 = *(const u32x4*)(wmod + (size_t)kB * 2688 + j0);
        c0 = bf2f(cvec[kB]);
        if (kB + 256 < D_) { w1 = *(const u32x4*)(wmod + (size_t)(kB + 256) * 2688 + j0); c1 = bf2f(cvec[kB + 256]); }
    }
    const bool doC = b < 224;
    const int ch = tid >> 8, n = tid & 255, dch = 2 * b + ch;
    float pAre = 0, pAim = 0, pBre = 0, pBim = 0, pCre = 0, pCim = 0, pdt = 0;
    if (doC) {
        const size_t o = (size_t)dch * NM + n;
        pAre = bf2f(Are[o]); pAim = bf2f(Aim[o]); pBre = bf2f(Bre[o]); pBim = bf2f(Bim[o]);
        pCre = bf2f(Cre[o]); pCim = bf2f(Cim[o]); pdt = bf2f(logdt[dch]);
    }

    if (doC) {
        const float dt = rbf(__expf(pdt));
        const float a = dt * pAre, bb = dt * pAim;
        const float dBr = dt * pBre, dBi = dt * pBim;
        cf W; W.re = pCre * dBr - pCim * dBi; W.im = pCre * dBi + pCim * dBr;
        const float a2 = a * 1.4426950408889634f, br = bb * 0.15915494309189535f;
        LDS char* tb = lds + ch * K1_TCH + n * 4;
#define PUT(row, v) (*(LDS unsigned*)(tb + (row) * K1_TROW) = pk2((v).re, (v).im))
#define PUTC(row, v) (*(LDS unsigned*)(tb + (row) * K1_TROW) = pk2((v).re, -(v).im))
        const cf z1 = zpow(a2, br, 1.f), z8 = zpow(a2, br, 8.f), z128 = zpow(a2, br, 128.f);
        const cf z256 = zpow(a2, br, 256.f), z512 = zpow(a2, br, 512.f), z1024 = zpow(a2, br, 1024.f);
        {
            cf q0; q0.re = 1.f; q0.im = 0.f;
            const cf q1 = z8, q2 = csq(q1), q3 = cmul(q2, q1), q4 = csq(q2), q5 = cmul(q4, q1), q6 = csq(q3), q7 = cmul(q4, q3);
            const cf q8 = csq(q4);
            PUTC(41, q0); PUTC(42, q1); PUTC(43, q2); PUTC(44, q3); PUTC(45, q4); PUTC(46, q5); PUTC(47, q6); PUTC(48, q7);
            PUTC(49, q8);
            cf t;
            t = cmul(q8, q1); PUTC(50, t); t = csq(q5); PUTC(51, t); t = cmul(q8, q3); PUTC(52, t);
            t = csq(q6); PUTC(53, t); t = cmul(q8, q5); PUTC(54, t); t = csq(q7); PUTC(55, t); t = cmul(q8, q7); PUTC(56, t);
        }
        {
            const cf z2 = csq(z1), z4 = csq(z2);
            cf P[16];
            P[0] = W; P[1] = cmul(W, z1); P[2] = cmul(W, z2); P[3] = cmul(P[2], z1);
            P[4] = cmul(W, z4); P[5] = cmul(P[4], z1); P[6] = cmul(P[4], z2); P[7] = cmul(P[6], z1);
#pragma unroll
            for (int c = 0; c < 8; ++c) P[8 + c] = cmul(P[c], z128);
#pragma unroll
            for (int c = 0; c < 16; ++c) PUT(c, P[c]);
            cf S1[8];
#pragma unroll
            for (int i = 0; i < 8; ++i) { S1[i] = cmul(P[8 * (i >> 2) + 2 * (i & 3)], z256); PUT(16 + i, S1[i]); }
            cf S2[8];
#pragma unroll
            for (int i = 0; i < 8; ++i) {
                const int a_ = i >> 1, c_ = i & 1;
                S2[i] = (a_ < 2) ? cmul(P[8 * a_ + 4 * c_], z512) : cmul(S1[4 * (a_ - 2) + 2 * c_], z512);
                PUT(24 + i, S2[i]);
            }
            cf S30;
#pragma unroll
            for (int a_ = 0; a_ < 8; ++a_) {
                const cf src = (a_ < 2) ? P[8 * a_] : (a_ < 4) ? S1[4 * (a_ - 2)] : S2[2 * (a_ - 4)];
                const cf t = cmul(src, z1024);
                if (a_ == 0) S30 = t;
                PUT(32 + a_, t);
            }
            const cf t = cmul(S30, z1024);
            PUT(40, t);
        }
#undef PUT
#undef PUTC
    }

    {
        float v[8];
        v[0] = bflo(xv.x); v[1] = bfhi(xv.x); v[2] = bflo(xv.y); v[3] = bfhi(xv.y);
        v[4] = bflo(xv.z); v[5] = bfhi(xv.z); v[6] = bflo(xv.w); v[7] = bfhi(xv.w);
        float s = 0.f;
#pragma unroll
        for (int i = 0; i < 8; ++i) s += v[i];
        const float mean = wave_sum(s) * (1.f / D_);
        float q = 0.f;
#pragma unroll
        for (int i = 0; i < 8; ++i) { const float t = v[i] - mean; q += t * t; }
        if (lane >= 56) q = 0.f;
        const float var = wave_sum(q) * (1.f / D_);
        if (lane == 0) {
            const float sd = rbf(sqrtf(rbf(rbf(var) + EPS)));
            *(f32x2*)(stats + 2 * (r0 + wid)) = f32x2{rbf(mean), sd};
        }
        if (lane < 56) *(LDS u32x4*)(lds + K1_OFF_XS + wid * 896 + lane * 16) = xv;
    }
    if (doB) {
        const float s0 = rbf(c0 / (1.f + __expf(-c0))), s1 = rbf(c1 / (1.f + __expf(-c1)));
        float p[8];
        p[0] = s0 * bflo(w0.x) + s1 * bflo(w1.x); p[1] = s0 * bfhi(w0.x) + s1 * bfhi(w1.x);
        p[2] = s0 * bflo(w0.y) + s1 * bflo(w1.y); p[3] = s0 * bfhi(w0.y) + s1 * bfhi(w1.y);
        p[4] = s0 * bflo(w0.z) + s1 * bflo(w1.z); p[5] = s0 * bfhi(w0.z) + s1 * bfhi(w1.z);
        p[6] = s0 * bflo(w0.w) + s1 * bflo(w1.w); p[7] = s0 * bfhi(w0.w) + s1 * bfhi(w1.w);
#pragma unroll
        for (int i = 0; i < 8; ++i) {
            const float t = wave_sum(p[i]);
            if (lane == 0) *(LDS float*)(lds + K1_OFF_MR + (wid * 8 + i) * 4) = t;
        }
    }
    __syncthreads();

    if (tid < D_) {
        u16 e[8];
#pragma unroll
        for (int i = 0; i < 8; ++i) e[i] = *(LDS u16*)(lds + K1_OFF_XS + i * 896 + tid * 2);
        u32x4 o;
        o.x = e[0] | ((unsigned)e[1] << 16); o.y = e[2] | ((unsigned)e[3] << 16);
        o.z = e[4] | ((unsigned)e[5] << 16); o.w = e[6] | ((unsigned)e[7] << 16);
        *(u32x4*)(xT + (size_t)tid * L_ + r0) = o;
    }
    if (doB && tid < 16) {
        const int hf = tid >> 3, i = tid & 7;
        float t = 0.f;
#pragma unroll
        for (int w = 0; w < 4; ++w) t += *(LDS float*)(lds + K1_OFF_MR + ((hf * 4 + w) * 8 + i) * 4);
        mod[16 * (b - 88) + tid] = rbf(t);
    }
    if (!doC) return;

    if (wid < 6) {
        const int c2 = wid / 3, tile = wid - 3 * c2;
        LDS char* tb = lds + c2 * K1_TCH;
        LDS char* pa = tb + (tile * 16 + (lane & 15)) * K1_TROW + (lane >> 4) * 16;
        LDS char* pb = tb + (41 + (lane & 15)) * K1_TROW + (lane >> 4) * 16;
        f32x4 acc = {0.f, 0.f, 0.f, 0.f};
#pragma unroll
        for (int s = 0; s < 16; ++s) {
            const bf16x8 a = as_frag(*(LDS u32x4*)(pa + s * 64));
            const bf16x8 bq = as_frag(*(LDS u32x4*)(pb + s * 64));
            acc = mfma16(a, bq, acc);
        }
        LDS float* kc = (LDS float*)(lds + K1_OFF_KC) + c2 * 768 + (tile * 16 + 4 * (lane >> 4)) * 16 + (lane & 15);
#pragma unroll
        for (int e = 0; e < 4; ++e) kc[e * 16] = acc[e];
    }
    __syncthreads();
    {
        LDS float* kc = (LDS float*)(lds + K1_OFF_KC) + ch * 768;
        float kv[8];
#pragma unroll
        for (int e = 0; e < 8; ++e) {
            const int m = 2047 - (8 * n + e);
            const int l = (int)rbf((float)m);
            const int hi7 = l >> 7;
            int row;
            if (l < 256) row = 8 * hi7 + (l & 7);
            else if (l < 512) row = 8 + 4 * hi7 + ((l >> 1) & 3);
            else if (l < 1024) row = 16 + 2 * hi7 + ((l >> 2) & 1);
            else row = 24 + hi7;
            kv[e] = kc[row * 16 + ((l >> 3) & 15)];
        }
        u32x4 o; o.x = pk2(kv[0], kv[1]); o.y = pk2(kv[2], kv[3]); o.z = pk2(kv[4], kv[5]); o.w = pk2(kv[6], kv[7]);
        *(u32x4*)(Rg + (size_t)dch * RG_STRIDE + 8 * n) = o;
        if (n < 4) *(u32x4*)(Rg + (size_t)dch * RG_STRIDE + 2048 + 8 * n) = u32x4{0, 0, 0, 0};
    }
}

constexpr int K2_RC = 4160;
constexpr int K2_RCH = 4 * K2_RC;
constexpr int K2_OFF_H = 2 * K2_RCH;
constexpr int K2_HCH = (256 + 2048) * 2;
constexpr int K2_OFF_RED = K2_OFF_H + 2 * K2_HCH;
constexpr int K2_OFF_WS = K2_OFF_RED + 8 * 2048;
constexpr int K2_OFF_WD = K2_OFF_WS + 224 * 64;
constexpr int K2_OFF_MD = K2_OFF_WD + 16 * 448;
constexpr int K2_OFF_UB = K2_OFF_MD + 2 * 224 * 4;
constexpr int K2_LDS = K2_OFF_UB + 8 * 64 * 4;

template <int TG>
DI void conv_phase(LDS char* pa, LDS char* pb, f32x4 (&acc)[4]) {
    constexpr int T0 = TG == 0 ? 0 : 1, T1 = TG == 0 ? 3 : 2, T2 = TG == 0 ? 4 : 5, T3 = TG == 0 ? 7 : 6;
    constexpr int NU = T3 + 1;
    bf16x8 A[NU], B[NU];
#pragma unroll
    for (int u = 0; u < NU; ++u) {
        const u32x2 lo = *(LDS u32x2*)(pa + 512 * (7 - u));
        const u32x2 hi = *(LDS u32x2*)(pa + 512 * (7 - u) + 8);
        A[u] = as_frag(u32x4{lo.x, lo.y, hi.x, hi.y});
        B[u] = as_frag(*(LDS u32x4*)(pb + 512 * u));
    }
#pragma unroll
    for (int u = 0; u < NU; ++u) {
        if (u <= T0) acc[0] = mfma16(A[u], B[T0 - u], acc[0]);
        if (u <= T1) acc[1] = mfma16(A[u], B[T1 - u], acc[1]);
        if (u <= T2) acc[2] = mfma16(A[u], B[T2 - u], acc[2]);
        acc[3] = mfma16(A[u], B[T3 - u], acc[3]);
    }
}

__global__ __launch_bounds__(512) void k2_conv(
    const u16* __restrict__ xT, const float* __restrict__ stats, const float* __restrict__ mod,
    const u16* __restrict__ Rg, const u16* __restrict__ dskip,
    const u16* __restrict__ wg, const u16* __restrict__ wu, const u16* __restrict__ wd,
    u16* __restrict__ x1T, u16* __restrict__ WpF, float* __restrict__ ub, u16* __restrict__ WdF)
{
    extern __shared__ __attribute__((aligned(16))) char smem[];
    LDS char* lds = (LDS char*)smem;
    const int tid = threadIdx.x, lane = tid & 63, wid = tid >> 6;
    const int b = (blockIdx.x & 7) * 28 + (blockIdx.x >> 3);
    const int ch = tid >> 8, i = tid & 255, d = 2 * b + ch;

    const u32x4 xv = *(const u32x4*)(xT + (size_t)d * L_ + 8 * i);
    const u32x4 rv0 = *(const u32x4*)(Rg + (size_t)d * RG_STRIDE + 8 * i);
    const u32x2 rv1 = *(const u32x2*)(Rg + (size_t)d * RG_STRIDE + 8 * i + 8);
    f32x4 st[4];
#pragma unroll
    for (int e = 0; e < 4; ++e) st[e] = *(const f32x4*)(stats + 16 * i + 4 * e);
    const float sa = mod[d], ca = mod[448 + d];
    const int fg = b >> 1, kh = b & 1;
    u32x4 wv[2];
#pragma unroll
    for (int r = 0; r < 2; ++r) {
        const int j = tid + 512 * r;
        wv[r] = u32x4{0, 0, 0, 0};
        if (j < 896) {
            const int row = j >> 2, mat = (j >> 1) & 1, hf = j & 1;
            const u16* src = (mat ? wu : wg) + (size_t)(224 * kh + row) * F_ + 16 * fg + 8 * hf;
            wv[r] = *(const u32x4*)src;
        }
    }
    u32x4 wdv = {0, 0, 0, 0};
    if (tid < 448) {
        const int row = tid / 28, cc = tid - row * 28;
        wdv = *(const u32x4*)(wd + (size_t)(16 * fg + row) * D_ + 224 * kh + 8 * cc);
    }
    float c1v = 0.f, sbv = 0.f;
    if (tid < 224) { c1v = rbf(1.f + mod[4 * 448 + 224 * kh + tid]); sbv = mod[3 * 448 + 224 * kh + tid]; }

    {
        const float c1 = rbf(1.f + ca);
        float xf[8];
        xf[0] = bflo(xv.x); xf[1] = bfhi(xv.x); xf[2] = bflo(xv.y); xf[3] = bfhi(xv.y);
        xf[4] = bflo(xv.z); xf[5] = bfhi(xv.z); xf[6] = bflo(xv.w); xf[7] = bfhi(xv.w);
        float h[8];
#pragma unroll
        for (int e = 0; e < 8; ++e) {
            const float m = st[e >> 1][2 * (e & 1)], sd = st[e >> 1][2 * (e & 1) + 1];
            const float nn = rbf(rbf(xf[e] - m) * __builtin_amdgcn_rcpf(sd));
            h[e] = rbf(nn * c1) + sa;
        }
        u32x4 o; o.x = pk2(h[0], h[1]); o.y = pk2(h[2], h[3]); o.z = pk2(h[4], h[5]); o.w = pk2(h[6], h[7]);
        *(LDS u32x4*)(lds + K2_OFF_H + ch * K2_HCH + 512 + 16 * i) = o;
        if (i < 32) *(LDS u32x4*)(lds + K2_OFF_H + ch * K2_HCH + 16 * i) = u32x4{0, 0, 0, 0};
    }
    {
        LDS char* rc = lds + ch * K2_RCH + 16 * i;
        const unsigned d0 = rv0.x, d1 = rv0.y, d2 = rv0.z, d3 = rv0.w, d4 = rv1.x, d5 = rv1.y;
        const unsigned a01 = __builtin_amdgcn_alignbit(d1, d0, 16), a12 = __builtin_amdgcn_alignbit(d2, d1, 16);
        const unsigned a23 = __builtin_amdgcn_alignbit(d3, d2, 16), a34 = __builtin_amdgcn_alignbit(d4, d3, 16);
        const unsigned a45 = __builtin_amdgcn_alignbit(d5, d4, 16);
        *(LDS u32x4*)(rc) = rv0;
        *(LDS u32x4*)(rc + K2_RC) = u32x4{a01, a12, a23, a34};
        *(LDS u32x4*)(rc + 2 * K2_RC) = u32x4{d1, d2, d3, d4};
        *(LDS u32x4*)(rc + 3 * K2_RC) = u32x4{a12, a23, a34, a45};
        if (i < 8) {
            *(LDS u32x4*)(lds + ch * K2_RCH + (i >> 1) * K2_RC + 16 * (256 + (i & 1))) = u32x4{0, 0, 0, 0};
        }
    }
#pragma unroll
    for (int r = 0; r < 2; ++r) {
        const int j = tid + 512 * r;
        if (j < 896) *(LDS u32x4*)(lds + K2_OFF_WS + (j >> 2) * 64 + (j & 3) * 16) = wv[r];
    }
    if (tid < 448) *(LDS u32x4*)(lds + K2_OFF_WD + tid * 16) = wdv;
    if (tid < 224) { *(LDS float*)(lds + K2_OFF_MD + tid * 4) = c1v; *(LDS float*)(lds + K2_OFF_MD + 896 + tid * 4) = sbv; }
    __syncthreads();

    const int wch = wid & 1, tg = (wid >> 1) & 1, pg = wid >> 2;
    const int bb = lane & 15, g = lane >> 4;
    f32x4 acc[4] = {};
    {
        LDS char* pa0 = lds + wch * K2_RCH + (3 - (bb & 3)) * K2_RC
                        + 2 * (2044 - 16 * (g >> 1) + 8 * (g & 1) - 4 * (bb >> 2) - 256 * 7);
        LDS char* pb0 = lds + K2_OFF_H + wch * K2_HCH + 2 * (256 + 16 * bb - 16 * (g >> 1) + 8 * (g & 1));
#pragma unroll
        for (int p = 0; p < 4; ++p) {
            const int phi = 2 * (4 * pg + p);
            if (tg == 0) conv_phase<0>(pa0 - 32 * phi, pb0 - 32 * phi, acc);
            else conv_phase<1>(pa0 - 32 * phi, pb0 - 32 * phi, acc);
        }
    }
    {
        LDS char* mine = lds + K2_OFF_RED + wid * 2048 + lane * 16;
        LDS char* theirs = lds + K2_OFF_RED + (wid ^ 4) * 2048 + lane * 16;
        *(LDS f32x4*)(mine) = pg ? acc[0] : acc[2];
        *(LDS f32x4*)(mine + 1024) = pg ? acc[1] : acc[3];
        __syncthreads();
        const f32x4 o0 = *(LDS f32x4*)(theirs), o1 = *(LDS f32x4*)(theirs + 1024);
        f32x4 y0 = (pg ? acc[2] : acc[0]) + o0, y1 = (pg ? acc[3] : acc[1]) + o1;
        const int dd = 2 * b + wch;
        const float ga = mod[2 * 448 + dd], dsk = bf2f(dskip[dd]);
#pragma unroll
        for (int k = 0; k < 2; ++k) {
            const int idx = 2 * pg + k;
            const int t = tg == 0 ? (idx == 0 ? 0 : idx == 1 ? 3 : idx == 2 ? 4 : 7) : (idx == 0 ? 1 : idx == 1 ? 2 : idx == 2 ? 5 : 6);
            const int l = 256 * t + 16 * bb + 4 * g;
            const f32x4 y = k ? y1 : y0;
            const u32x2 hv = *(LDS u32x2*)(lds + K2_OFF_H + wch * K2_HCH + 512 + 2 * l);
            const u32x2 xr = *(const u32x2*)(xT + (size_t)dd * L_ + l);
            float hf[4] = {bflo(hv.x), bfhi(hv.x), bflo(hv.y), bfhi(hv.y)};
            float xf[4] = {bflo(xr.x), bfhi(xr.x), bflo(xr.y), bfhi(xr.y)};
            float o[4];
#pragma unroll
            for (int e = 0; e < 4; ++e) {
                const float s = rbf(rbf(y[e]) + rbf(hf[e] * dsk));
                o[e] = xf[e] + rbf(ga * s);
            }
            *(u32x2*)(x1T + (size_t)dd * L_ + l) = u32x2{pk2(o[0], o[1]), pk2(o[2], o[3])};
        }
    }

    {
        const int h = lane >> 5, blk = (lane >> 4) & 1, q = (lane & 15) >> 2, p = lane & 3;
        float us = 0.f, bs = 0.f;
#pragma unroll
        for (int r = 0; r < 2; ++r) {
            const int s = wid + 8 * r;
            if (s < 14) {
                LDS char* a0 = lds + K2_OFF_WS + (16 * s + 8 * h + q) * 64 + 32 * blk + 8 * p;
                const u32x4 w = __builtin_bit_cast(u32x4, tr_frag(a0, a0 + 256));
                const f32x4 ca0 = *(LDS f32x4*)(lds + K2_OFF_MD + (16 * s + 8 * h) * 4);
                const f32x4 ca1 = *(LDS f32x4*)(lds + K2_OFF_MD + (16 * s + 8 * h + 4) * 4);
                const f32x4 sb0 = *(LDS f32x4*)(lds + K2_OFF_MD + 896 + (16 * s + 8 * h) * 4);
                const f32x4 sb1 = *(LDS f32x4*)(lds + K2_OFF_MD + 896 + (16 * s + 8 * h + 4) * 4);
                float wf[8] = {bflo(w.x), bfhi(w.x), bflo(w.y), bfhi(w.y), bflo(w.z), bfhi(w.z), bflo(w.w), bfhi(w.w)};
                u32x4 o;
                o.x = pk2(wf[0] * ca0[0], wf[1] * ca0[1]); o.y = pk2(wf[2] * ca0[2], wf[3] * ca0[3]);
                o.z = pk2(wf[4] * ca1[0], wf[5] * ca1[1]); o.w = pk2(wf[6] * ca1[2], wf[7] * ca1[3]);
                *(u32x4*)(WpF + ((size_t)(fg * 28 + 14 * kh + s) * 64 + lane) * 8) = o;
                us += bflo(o.x) + bfhi(o.x) + bflo(o.y) + bfhi(o.y) + bflo(o.z) + bfhi(o.z) + bflo(o.w) + bfhi(o.w);
                bs += wf[0] * sb0[0] + wf[1] * sb0[1] + wf[2] * sb0[2] + wf[3] * sb0[3]
                    + wf[4] * sb1[0] + wf[5] * sb1[1] + wf[6] * sb1[2] + wf[7] * sb1[3];
            }
        }
        us += __int_as_float(__builtin_amdgcn_ds_bpermute((lane ^ 32) << 2, __float_as_int(us)));
        bs += __int_as_float(__builtin_amdgcn_ds_bpermute((lane ^ 32) << 2, __float_as_int(bs)));
        if (lane < 32) {
            *(LDS float*)(lds + K2_OFF_UB + (wid * 64 + lane) * 4) = us;
            *(LDS float*)(lds + K2_OFF_UB + (wid * 64 + 32 + lane) * 4) = bs;
        }
        if (wid < 7) {
            const int dg = 7 * kh + wid;
            LDS char* a0 = lds + K2_OFF_WD + (4 * h + q) * 448 + (32 * wid + 16 * blk + 4 * p) * 2;
            const u32x4 w = __builtin_bit_cast(u32x4, tr_frag(a0, a0 + 8 * 448));
            *(u32x4*)(WdF + ((size_t)(dg * 112 + fg) * 64 + lane) * 8) = w;
        }
        __syncthreads();
        if (tid < 64) {
            float t = 0.f;
#pragma unroll
            for (int w = 0; w < 8; ++w) t += *(LDS float*)(lds + K2_OFF_UB + (w * 64 + tid) * 4);
            ub[(kh * 2 + (tid >> 5)) * 3584 + fg * 32 + (tid & 31)] = t;
        }
    }
}

template <int OFF>
DI void gload16(u32x4& dst, unsigned voff, const void* sbase) {
    asm volatile("global_load_dwordx4 %0, %1, %2 offset:%3" : "=v"(dst) : "v"(voff), "s"(sbase), "i"(OFF));
}
DI void gload4(float& dst, unsigned voff, const void* sbase) {
    asm volatile("global_load_dword %0, %1, %2" : "=v"(dst) : "v"(voff), "s"(sbase));
}
DI void glds16(unsigned voff, const void* sbase, unsigned ldsdst) {
    unsigned keep;
    asm volatile("s_mov_b32 %0, m0\n\ts_mov_b32 m0, %3\n\ts_nop 0\n\tglobal_load_lds_dwordx4 %1, %2\n\ts_mov_b32 m0, %0"
                 : "=&s"(keep) : "v"(voff), "s"(sbase), "s"(ldsdst) : "memory");
}
template <int N>
DI void wait_vm4(u32x4& a, u32x4& b, u32x4& c, u32x4& d) {
    asm volatile("s_waitcnt vmcnt(%4)" : "+v"(a), "+v"(b), "+v"(c), "+v"(d) : "i"(N) : "memory");
}
template <int N>
DI void wait_vm4_bar(u32x4& a, u32x4& b, u32x4& c, u32x4& d) {
    asm volatile("s_waitcnt vmcnt(%4)\n\ts_barrier" : "+v"(a), "+v"(b), "+v"(c), "+v"(d) : "i"(N) : "memory");
}

constexpr int K3_OFF_ST = 448 * 256;
constexpr int K3_OFF_RS = K3_OFF_ST + 2 * 128 * 8;
constexpr int K3_OFF_UB = K3_OFF_RS + 128 * 8;
constexpr int K3_LDS = K3_OFF_UB + 512 * 4;

template <int C>
DI void k3_issue(u32x4 (&wf)[28], unsigned voffA, const char* baseA, unsigned ldsA, unsigned voffW, const char* wE, const char* wO) {
    glds16(voffA, baseA + (size_t)(64 * C) * (L_ * 2), ldsA + (64 * C) * 256);
    glds16(voffA, baseA + (size_t)(64 * C + 32) * (L_ * 2), ldsA + (64 * C + 32) * 256);
    gload16<0>(wf[4 * C], voffW, wE + 4096 * C);
    gload16<1024>(wf[4 * C + 1], voffW, wO + 4096 * C);
    gload16<2048>(wf[4 * C + 2], voffW, wE + 4096 * C);
    gload16<3072>(wf[4 * C + 3], voffW, wO + 4096 * C);
}
template <int C>
DI void k3_chunk(u32x4 (&wf)[28], f32x16 (&acc)[4], LDS char* (&pE)[4], LDS char* (&pO)[4], float& ssum, float& ssq) {
    wait_vm4_bar<6 * (6 - C)>(wf[4 * C], wf[4 * C + 1], wf[4 * C + 2], wf[4 * C + 3]);
    const bf16x2 ones = {(__bf16)1.f, (__bf16)1.f};
#pragma unroll
    for (int j = 0; j < 4; ++j) {
        const int s = 4 * C + j;
#pragma unroll
        for (int t = 0; t < 4; ++t) {
            LDS char* p = ((j & 1) ? pO[t] : pE[t]) + s * 4096;
            const bf16x8 xb = tr_frag(p, p + 1024);
            acc[t] = mfma32(as_frag(wf[s]), xb, acc[t]);
            if (t == 0 && (j & 1) == 0) {
                const u32x4 xu = __builtin_bit_cast(u32x4, xb);
                const unsigned x0 = xu.x, x1 = xu.y, x2 = xu.z, x3 = xu.w;
                const bf16x2 v0 = __builtin_bit_cast(bf16x2, x0), v1 = __builtin_bit_cast(bf16x2, x1);
                const bf16x2 v2 = __builtin_bit_cast(bf16x2, x2), v3 = __builtin_bit_cast(bf16x2, x3);
                ssum = __builtin_amdgcn_fdot2_f32_bf16(v0, ones, ssum, false); ssq = __builtin_amdgcn_fdot2_f32_bf16(v0, v0, ssq, false);
                ssum = __builtin_amdgcn_fdot2_f32_bf16(v1, ones, ssum, false); ssq = __builtin_amdgcn_fdot2_f32_bf16(v1, v1, ssq, false);
                ssum = __builtin_amdgcn_fdot2_f32_bf16(v2, ones, ssum, false); ssq = __builtin_amdgcn_fdot2_f32_bf16(v2, v2, ssq, false);
                ssum = __builtin_amdgcn_fdot2_f32_bf16(v3, ones, ssum, false); ssq = __builtin_amdgcn_fdot2_f32_bf16(v3, v3, ssq, false);
            }
        }
    }
}

__global__ __launch_bounds__(512) void k3_gemm1(
    const u16* __restrict__ x1T, const u16* __restrict__ WpF, const float* __restrict__ ub, u16* __restrict__ hF)
{
    extern __shared__ __attribute__((aligned(16))) char smem[];
    LDS char* lds = (LDS char*)smem;
    const int tid = threadIdx.x, lane = tid & 63;
    const int wid = __builtin_amdgcn_readfirstlane(tid >> 6);
    const int xc = blockIdx.x & 7, q_ = blockIdx.x >> 3;
    const int mi = 4 * (xc >> 1) + (q_ & 3), ni = 7 * (xc & 1) + (q_ >> 2);
    const int l0 = 128 * mi, fg = 8 * ni + wid;
    const int wq = wid & 3, par = wid >> 2;

    float ub0, ub1;
    gload4(ub0, (tid & 255) * 4, (const char*)ub + (size_t)(par * 3584 + ni * 256) * 4);
    gload4(ub1, (tid & 255) * 4, (const char*)ub + (size_t)((2 + par) * 3584 + ni * 256) * 4);
    u32x4 wf[28];
    {
        const int rr = lane >> 4, cc = (lane & 15) ^ (rr << 2);
        const unsigned voffA = (unsigned)(rr * L_ + 8 * cc) * 2;
        const char* baseA = (const char*)x1T + ((size_t)(4 * wid) * L_ + l0) * 2;
        const unsigned ldsA = (unsigned)(size_t)lds + 4 * wid * 256;
        const char* wb = (const char*)WpF + (size_t)fg * 28 * 1024;
        const char* wE = wb + par * 1024;
        const char* wO = wb - par * 1024;
        const unsigned voffW = lane * 16;
        k3_issue<0>(wf, voffA, baseA, ldsA, voffW, wE, wO); k3_issue<1>(wf, voffA, baseA, ldsA, voffW, wE, wO);
        k3_issue<2>(wf, voffA, baseA, ldsA, voffW, wE, wO); k3_issue<3>(wf, voffA, baseA, ldsA, voffW, wE, wO);
        k3_issue<4>(wf, voffA, baseA, ldsA, voffW, wE, wO); k3_issue<5>(wf, voffA, baseA, ldsA, voffW, wE, wO);
        k3_issue<6>(wf, voffA, baseA, ldsA, voffW, wE, wO);
    }
    f32x16 acc[4] = {};
    float ssum = 0.f, ssq = 0.f;
    const int h = lane >> 5, blk = (lane >> 4) & 1, q = (lane & 15) >> 2, p = lane & 3;
    LDS char* pE[4]; LDS char* pO[4];
#pragma unroll
    for (int t = 0; t < 4; ++t) {
        LDS char* pb = lds + (8 * h + q) * 256 + 64 * ((t ^ wq) ^ q) + 32 * blk + 8 * p;
        pE[t] = pb + par * 4096; pO[t] = pb - par * 4096;
    }
    k3_chunk<0>(wf, acc, pE, pO, ssum, ssq); k3_chunk<1>(wf, acc, pE, pO, ssum, ssq);
    k3_chunk<2>(wf, acc, pE, pO, ssum, ssq); k3_chunk<3>(wf, acc, pE, pO, ssum, ssq);
    k3_chunk<4>(wf, acc, pE, pO, ssum, ssq); k3_chunk<5>(wf, acc, pE, pO, ssum, ssq);
    k3_chunk<6>(wf, acc, pE, pO, ssum, ssq);

    asm volatile("" : "+v"(ub0), "+v"(ub1));
    *(LDS float*)(lds + K3_OFF_UB + tid * 4) = ub0 + ub1;
    ssum += __int_as_float(__builtin_amdgcn_ds_bpermute((lane ^ 32) << 2, __float_as_int(ssum)));
    ssq += __int_as_float(__builtin_amdgcn_ds_bpermute((lane ^ 32) << 2, __float_as_int(ssq)));
    if (lane < 32) *(LDS f32x2*)(lds + K3_OFF_ST + (par * 128 + wq * 32 + lane) * 8) = f32x2{ssum, ssq};
    __syncthreads();
    if (tid < 128) {
        const f32x2 v0 = *(LDS f32x2*)(lds + K3_OFF_ST + tid * 8), v1 = *(LDS f32x2*)(lds + K3_OFF_ST + (128 + tid) * 8);
        const float mean = (v0.x + v1.x) * (1.f / D_);
        const float var = fmaxf((v0.y + v1.y) * (1.f / D_) - mean * mean, 0.f);
        const float sd = rbf(sqrtf(rbf(rbf(var) + EPS)));
        *(LDS f32x2*)(lds + K3_OFF_RS + tid * 8) = f32x2{rbf(mean), 1.f / sd};
    }
    __syncthreads();
    f32x4 ug[2], bg[2], uu[2], bu[2];
#pragma unroll
    for (int k = 0; k < 2; ++k) {
        LDS char* pu = lds + K3_OFF_UB + (wid * 32 + 8 * k + 4 * h) * 4;
        ug[k] = *(LDS f32x4*)(pu); uu[k] = *(LDS f32x4*)(pu + 64);
        bg[k] = *(LDS f32x4*)(pu + 1024); bu[k] = *(LDS f32x4*)(pu + 1024 + 64);
    }
#pragma unroll
    for (int t = 0; t < 4; ++t) {
        const int ta = t ^ wq;
        const f32x2 ms = *(LDS f32x2*)(lds + K3_OFF_RS + (32 * ta + (lane & 31)) * 8);
        const float m = ms.x, r_ = ms.y;
        float hv[8];
#pragma unroll
        for (int r = 0; r < 8; ++r) {
            const float gg = rbf(r_ * (acc[t][r] - m * ug[r >> 2][r & 3]) + bg[r >> 2][r & 3]);
            const float up = rbf(r_ * (acc[t][r + 8] - m * uu[r >> 2][r & 3]) + bu[r >> 2][r & 3]);
            hv[r] = gg * up;
        }
        const u32x4 o = {pk2(hv[0], hv[1]), pk2(hv[2], hv[3]), pk2(hv[4], hv[5]), pk2(hv[6], hv[7])};
        *(u32x4*)(hF + ((size_t)((4 * mi + ta) * 112 + fg) * 64 + lane) * 8) = o;
    }
}

constexpr int K4_OFF_X = 4 * 3 * 4096;
constexpr int K4_LDS = K4_OFF_X + 64 * 128;
constexpr int K4_PF = 12;

template <int S>
DI void k4_load(u32x4 (&w0)[K4_PF], u32x4 (&w1)[K4_PF], u32x4 (&h0)[K4_PF], u32x4 (&h1)[K4_PF], unsigned voff,
                const char* pw0, const char* pw1, const char* ph0, const char* ph1) {
    constexpr int j = S % K4_PF, hi = (S >> 2) * 4096, lo = (S & 3) * 1024;
    gload16<lo>(w0[j], voff, pw0 + hi);
    gload16<lo>(h0[j], voff, ph0 + hi);
    gload16<lo>(w1[j], voff, pw1 + hi);
    gload16<lo>(h1[j], voff, ph1 + hi);
}
template <int S>
DI void k4_steps(u32x4 (&w0)[K4_PF], u32x4 (&w1)[K4_PF], u32x4 (&h0)[K4_PF], u32x4 (&h1)[K4_PF], f32x16 (&acc)[2][2],
                 unsigned voff, const char* pw0, const char* pw1, const char* ph0, const char* ph1) {
    if constexpr (S < 28) {
        constexpr int j = S % K4_PF;
        constexpr int younger = (27 - S < K4_PF - 1) ? 27 - S : K4_PF - 1;
        wait_vm4<4 * younger>(w0[j], w1[j], h0[j], h1[j]);
        const bf16x8 a0 = as_frag(w0[j]), a1 = as_frag(w1[j]), b0 = as_frag(h0[j]), b1 = as_frag(h1[j]);
        acc[0][0] = mfma32(a0, b0, acc[0][0]);
        acc[1][0] = mfma32(a1, b0, acc[1][0]);
        acc[0][1] = mfma32(a0, b1, acc[0][1]);
        acc[1][1] = mfma32(a1, b1, acc[1][1]);
        if constexpr (S + K4_PF < 28) {
            __builtin_amdgcn_sched_barrier(0);
            k4_load<S + K4_PF>(w0, w1, h0, h1, voff, pw0, pw1, ph0, ph1);
        }
        k4_steps<S + 1>(w0, w1, h0, h1, acc, voff, pw0, pw1, ph0, ph1);
    }
}
template <int S>
DI void k4_prologue(u32x4 (&w0)[K4_PF], u32x4 (&w1)[K4_PF], u32x4 (&h0)[K4_PF], u32x4 (&h1)[K4_PF], unsigned voff,
                    const char* pw0, const char* pw1, const char* ph0, const char* ph1) {
    if constexpr (S < K4_PF) {
        k4_load<S>(w0, w1, h0, h1, voff, pw0, pw1, ph0, ph1);
        k4_prologue<S + 1>(w0, w1, h0, h1, voff, pw0, pw1, ph0, ph1);
    }
}

__global__ __launch_bounds__(256) void k4_gemm2(
    const u16* __restrict__ hF, const u16* __restrict__ WdF, const u16* __restrict__ x1T,
    const float* __restrict__ mod, u16* __restrict__ out)
{
    extern __shared__ __attribute__((aligned(16))) char smem[];
    LDS char* lds = (LDS char*)smem;
    const int tid = threadIdx.x, lane = tid & 63;
    const int wid = __builtin_amdgcn_readfirstlane(tid >> 6);
    const int xc = blockIdx.x & 7, q_ = blockIdx.x >> 3;
    const int mi = 4 * xc + (q_ & 3), ni = q_ >> 2;
    const char* pw0 = (const char*)WdF + (size_t)((2 * ni) * 112 + 28 * wid) * 1024;
    const char* pw1 = pw0 + (size_t)112 * 1024;
    const char* ph0 = (const char*)hF + (size_t)((2 * mi) * 112 + 28 * wid) * 1024;
    const char* ph1 = ph0 + (size_t)112 * 1024;
    const int h = lane >> 5;

    {
        const unsigned voffX = (unsigned)((lane >> 3) * L_ + 8 * (lane & 7)) * 2;
        const char* bx = (const char*)x1T + ((size_t)(64 * ni + 16 * wid) * L_ + 64 * mi) * 2;
        glds16(voffX, bx, (unsigned)(size_t)lds + K4_OFF_X + 16 * wid * 128);
        glds16(voffX, bx + (size_t)8 * L_ * 2, (unsigned)(size_t)lds + K4_OFF_X + (16 * wid + 8) * 128);
    }
    u32x4 gbv[4];
    {
        const char* bg = (const char*)mod + (5 * 448 + 64 * ni + 32 * (wid >> 1)) * 4;
        gload16<0>(gbv[0], h * 16, bg); gload16<32>(gbv[1], h * 16, bg); gload16<64>(gbv[2], h * 16, bg); gload16<96>(gbv[3], h * 16, bg);
    }
    f32x16 acc[2][2] = {};
    u32x4 w0[K4_PF], w1[K4_PF], h0[K4_PF], h1[K4_PF];
    const unsigned voff = lane * 16;
    k4_prologue<0>(w0, w1, h0, h1, voff, pw0, pw1, ph0, ph1);
    k4_steps<0>(w0, w1, h0, h1, acc, voff, pw0, pw1, ph0, ph1);
    asm volatile("" : "+v"(gbv[0]), "+v"(gbv[1]), "+v"(gbv[2]), "+v"(gbv[3]));

#pragma unroll
    for (int t = 0; t < 4; ++t) {
        if (t != wid) {
            LDS char* dst = lds + (wid * 3 + (t > wid ? t - 1 : t)) * 4096 + lane * 16;
            const f32x16 v = acc[t >> 1][t & 1];
#pragma unroll
            for (int g = 0; g < 4; ++g) *(LDS f32x4*)(dst + g * 1024) = f32x4{v[4 * g], v[4 * g + 1], v[4 * g + 2], v[4 * g + 3]};
        }
    }
    __syncthreads();
    f32x16 y = wid == 0 ? acc[0][0] : wid == 1 ? acc[0][1] : wid == 2 ? acc[1][0] : acc[1][1];
#pragma unroll
    for (int s = 0; s < 4; ++s) {
        if (s != wid) {
            LDS char* src = lds + (s * 3 + (wid > s ? wid - 1 : wid)) * 4096 + lane * 16;
#pragma unroll
            for (int g = 0; g < 4; ++g) {
                const f32x4 v = *(LDS f32x4*)(src + g * 1024);
                y[4 * g] += v[0]; y[4 * g + 1] += v[1]; y[4 * g + 2] += v[2]; y[4 * g + 3] += v[3];
            }
        }
    }
    const int dbase = 64 * ni + 32 * (wid >> 1), lrow = 64 * mi + 32 * (wid & 1) + (lane & 31);
    const int blk = (lane >> 4) & 1, q = (lane & 15) >> 2, p = lane & 3;
#pragma unroll
    for (int g = 0; g < 4; ++g) {
        LDS char* px = lds + K4_OFF_X + (32 * (wid >> 1) + 8 * g + 4 * h + q) * 128 + (32 * (wid & 1) + 16 * blk + 4 * p) * 2;
        const s16x4 xr = __builtin_amdgcn_ds_read_tr16_b64_v4i16((LDS s16x4*)px);
        const f32x4 gb = __builtin_bit_cast(f32x4, gbv[g]);
        float o[4];
#pragma unroll
        for (int e = 0; e < 4; ++e) o[e] = bf2f((u16)xr[e]) + rbf(gb[e] * rbf(y[4 * g + e]));
        *(u32x2*)(out + (size_t)lrow * D_ + dbase + 8 * g + 4 * h) = u32x2{pk2(o[0], o[1]), pk2(o[2], o[3])};
    }
}

extern "C" void kernel_launch(void* const* d_in, const int* in_sizes, int n_in,
                              void* d_out, int out_size, void* d_ws, size_t ws_size,
                              hipStream_t stream) {
    const u16* x = (const u16*)d_in[0];
    const u16* Are = (const u16*)d_in[1];
    const u16* Aim = (const u16*)d_in[2];
    const u16* Bre = (const u16*)d_in[3];
    const u16* Bim = (const u16*)d_in[4];
    const u16* Cre = (const u16*)d_in[5];
    const u16* Cim = (const u16*)d_in[6];
    const u16* logdt = (const u16*)d_in[7];
    const u16* dskip = (const u16*)d_in[8];
    const u16* wg = (const u16*)d_in[10];
    const u16* wu = (const u16*)d_in[11];
    const u16* wd = (const u16*)d_in[12];
    const u16* cvec = (const u16*)d_in[13];
    const u16* wmod = (const u16*)d_in[14];
    char* ws = (char*)d_ws;
    u16* xT = (u16*)(ws + WS_XT);
    u16* x1T = (u16*)(ws + WS_X1T);
    float* stats = (float*)(ws + WS_STATS);
    float* mod = (float*)(ws + WS_MOD);
    u16* Rg = (u16*)(ws + WS_RG);
    u16* WpF = (u16*)(ws + WS_WPF);
    float* ub = (float*)(ws + WS_UB);
    u16* WdF = (u16*)(ws + WS_WDF);
    u16* hF = (u16*)(ws + WS_HF);

    (void)hipFuncSetAttribute((const void*)k1_prep, hipFuncAttributeMaxDynamicSharedMemorySize, K1_LDS);
    (void)hipFuncSetAttribute((const void*)k2_conv, hipFuncAttributeMaxDynamicSharedMemorySize, K2_LDS);
    (void)hipFuncSetAttribute((const void*)k3_gemm1, hipFuncAttributeMaxDynamicSharedMemorySize, K3_LDS);
    (void)hipFuncSetAttribute((const void*)k4_gemm2, hipFuncAttributeMaxDynamicSharedMemorySize, K4_LDS);
    k1_prep<<<256, 512, K1_LDS, stream>>>(x, Are, Aim, Bre, Bim, Cre, Cim, logdt, cvec, wmod, xT, stats, mod, Rg);
    k2_conv<<<224, 512, K2_LDS, stream>>>(xT, stats, mod, Rg, dskip, wg, wu, wd, x1T, WpF, ub, WdF);
    k3_gemm1<<<224, 512, K3_LDS, stream>>>(x1T, WpF, ub, hF);
    k4_gemm2<<<224, 256, K4_LDS, stream>>>(hF, WdF, x1T, mod, (u16*)d_out);
}
```

```cpp
#include <hip/hip_runtime.h>
#include <hip/hip_bf16.h>
#include <stdint.h>

typedef unsigned short u16;
typedef unsigned long long u64;
typedef __attribute__((ext_vector_type(8))) __bf16 bf16x8;
typedef __attribute__((ext_vector_type(2))) __bf16 bf16x2;
typedef __attribute__((ext_vector_type(4))) short s16x4;
typedef __attribute__((ext_vector_type(4))) float f32x4;
typedef __attribute__((ext_vector_type(2))) float f32x2;
typedef __attribute__((ext_vector_type(16))) float f32x16;
typedef __attribute__((ext_vector_type(4))) unsigned u32x4;
typedef __attribute__((ext_vector_type(2))) unsigned u32x2;
#define LDS __attribute__((address_space(3)))
#define DI __device__ __forceinline__

constexpr int L_ = 2048, D_ = 448, NM = 256, F_ = 1792;
constexpr float EPS = 1e-5f;

constexpr size_t WS_EP = 0;
constexpr size_t WS_FA = 4096;
constexpr size_t WS_FB = WS_FA + 8 * 2048;
constexpr size_t WS_FC = WS_FB + 8 * 2048;
constexpr size_t WS_FD = WS_FC + 8 * 2048;
constexpr size_t WS_STATS = WS_FD + 224 * 128;
constexpr size_t WS_MOD = WS_STATS + L_ * 4;
constexpr size_t WS_UB = WS_MOD + 4 * 2688 * 4;
constexpr size_t WS_XT = WS_UB + 2 * 3584 * 4;
constexpr size_t WS_X1T = WS_XT + (size_t)D_ * L_ * 2;
constexpr size_t WS_WPF = WS_X1T + (size_t)D_ * L_ * 2;
constexpr size_t WS_WDF = WS_WPF + (size_t)112 * 28 * 1024;
constexpr size_t WS_HF = WS_WDF + (size_t)14 * 112 * 1024;
constexpr size_t WS_END = WS_HF + (size_t)64 * 112 * 1024;
static_assert(WS_STATS % 256 == 0 && WS_MOD % 256 == 0 && WS_UB % 128 == 0 && WS_X1T % 128 == 0, "alignment");

constexpr int T_ROW = 1056;
constexpr int T_CH = 57 * T_ROW;
constexpr int O_KC = 2 * T_CH;
constexpr int KC_CH = 772 * 4;
constexpr int O_MR = O_KC + 2 * KC_CH;
constexpr int O_SB = O_MR + 2048;
constexpr int O_OK = O_SB + 64;
constexpr int O_WS = O_OK + 64;
constexpr int O_MD = O_WS + 16 * 896;
constexpr int O_GB = O_MD + 64;
constexpr int LDS_BYTES = 147456;
static_assert(O_GB + 256 <= LDS_BYTES && O_GB >= 131072 + 8192, "lds map");
constexpr int RC = 4160, RCH = 8 * RC;
constexpr int O_H = 2 * RCH;
constexpr int HCH = (256 + 2048) * 2;
constexpr int O_XR = O_H + 2 * HCH;
constexpr int O_RED = O_XR + 2 * 4096;
static_assert(O_RED + 16384 <= O_KC, "conv buffers must not reach the K table");
constexpr int O_ST = 448 * 256;
constexpr int O_RS = O_ST + 2048;
constexpr int O_UBS = O_RS + 1024;
constexpr int O_X2 = 131072;
static_assert(O_UBS + 2048 <= O_X2 && O_X2 + 8192 <= LDS_BYTES, "lds map");

DI float bf2f(unsigned h) { return __uint_as_float(h << 16); }
DI float bflo(unsigned u) { return __uint_as_float(u << 16); }
DI float bfhi(unsigned u) { return __uint_as_float(u & 0xffff0000u); }
DI unsigned pk2(float a, float b) { bf16x2 r = {(__bf16)a, (__bf16)b}; return __builtin_bit_cast(unsigned, r); }
DI float rbf(float a) { return (float)(__bf16)a; }
DI bf16x2 asb2(unsigned u) { return __builtin_bit_cast(bf16x2, u); }
DI float dot2(unsigned a, unsigned b, float c) { return __builtin_amdgcn_fdot2_f32_bf16(asb2(a), asb2(b), c, false); }

template <int CTRL, int RM>
DI float dpp_add(float v) {
    int t = __builtin_amdgcn_update_dpp(0, __float_as_int(v), CTRL, RM, 0xf, true);
    return v + __int_as_float(t);
}
DI float wave_sum(float v) {
    v = dpp_add<0x111, 0xf>(v);
    v = dpp_add<0x112, 0xf>(v);
    v = dpp_add<0x114, 0xf>(v);
    v = dpp_add<0x118, 0xf>(v);
    v = dpp_add<0x142, 0xa>(v);
    v = dpp_add<0x143, 0xc>(v);
    return __int_as_float(__builtin_amdgcn_readlane(__float_as_int(v), 63));
}
DI float swap32_add(float a, float b) {
    auto r = __builtin_amdgcn_permlane32_swap(__float_as_uint(a), __float_as_uint(b), false, false);
    return __uint_as_float(r[0]) + __uint_as_float(r[1]);
}
DI float swap16_add(float a, float b) {
    auto r = __builtin_amdgcn_permlane16_swap(__float_as_uint(a), __float_as_uint(b), false, false);
    return __uint_as_float(r[0]) + __uint_as_float(r[1]);
}
DI float half_add(float a) {
    auto r = __builtin_amdgcn_permlane32_swap(__float_as_uint(a), __float_as_uint(a), false, false);
    return __uint_as_float(r[0]) + __uint_as_float(r[1]);
}
DI f32x4 mfma16(bf16x8 a, bf16x8 b, f32x4 c) { return __builtin_amdgcn_mfma_f32_16x16x32_bf16(a, b, c, 0, 0, 0); }
DI f32x16 mfma32(bf16x8 a, bf16x8 b, f32x16 c) { return __builtin_amdgcn_mfma_f32_32x32x16_bf16(a, b, c, 0, 0, 0); }
DI bf16x8 as_frag(u32x4 v) { return __builtin_bit_cast(bf16x8, v); }
DI bf16x8 tr_frag(LDS char* p0, LDS char* p1) {
    s16x4 a = __builtin_amdgcn_ds_read_tr16_b64_v4i16((LDS s16x4*)p0);
    s16x4 b = __builtin_amdgcn_ds_read_tr16_b64_v4i16((LDS s16x4*)p1);
    return __builtin_bit_cast(bf16x8, __builtin_shufflevector(a, b, 0, 1, 2, 3, 4, 5, 6, 7));
}
#define SB0() __builtin_amdgcn_sched_barrier(0)

#define LD_ASM(name, inst, T)                                                                                   \
    template <int OFF = 0> DI void name(T& dst, unsigned voff, const void* sbase) {                             \
        asm volatile(inst " %0, %1, %2 offset:%3" : "=v"(dst) : "v"(voff), "s"(sbase), "i"(OFF));               \
    }                                                                                                           \
    template <int OFF = 0> DI void name##_c(T& dst, unsigned voff, const void* sbase) {                         \
        asm volatile(inst " %0, %1, %2 offset:%3 sc1" : "=v"(dst) : "v"(voff), "s"(sbase), "i"(OFF));           \
    }
LD_ASM(ld16, "global_load_dwordx4", u32x4)
LD_ASM(ld8, "global_load_dwordx2", u32x2)
LD_ASM(ld4, "global_load_dword", unsigned)
LD_ASM(ld2, "global_load_ushort", unsigned)
DI void ld4_c_io(unsigned& dst, unsigned voff, const void* sbase) {
    asm volatile("global_load_dword %0, %1, %2 sc1" : "+v"(dst) : "v"(voff), "s"(sbase));
}
DI void ld16v(u32x4& dst, const void* p) { asm volatile("global_load_dwordx4 %0, %1, off" : "=v"(dst) : "v"(p)); }
DI void st16_c(unsigned voff, const void* sbase, u32x4 v) {
    asm volatile("global_store_dwordx4 %0, %1, %2 sc1\n\ts_nop 1" :: "v"(voff), "v"(v), "s"(sbase) : "memory");
}
DI void st8_c(unsigned voff, const void* sbase, u32x2 v) {
    asm volatile("global_store_dwordx2 %0, %1, %2 sc1\n\ts_nop 1" :: "v"(voff), "v"(v), "s"(sbase) : "memory");
}
DI void st4_c(unsigned voff, const void* sbase, unsigned v) {
    asm volatile("global_store_dword %0, %1, %2 sc1\n\ts_nop 1" :: "v"(voff), "v"(v), "s"(sbase) : "memory");
}
DI void glds16_c(unsigned voff, const void* sbase, unsigned ldsdst) {
    unsigned keep;
    asm volatile("s_mov_b32 %0, m0\n\ts_mov_b32 m0, %3\n\ts_nop 0\n\tglobal_load_lds_dwordx4 %1, %2 sc1\n\ts_mov_b32 m0, %0"
                 : "=&s"(keep) : "v"(voff), "s"(sbase), "s"(ldsdst) : "memory");
}
DI void glds16(unsigned voff, const void* sbase, unsigned ldsdst) {
    unsigned keep;
    asm volatile("s_mov_b32 %0, m0\n\ts_mov_b32 m0, %3\n\ts_nop 0\n\tglobal_load_lds_dwordx4 %1, %2\n\ts_mov_b32 m0, %0"
                 : "=&s"(keep) : "v"(voff), "s"(sbase), "s"(ldsdst) : "memory");
}
template <int N> DI void wait_vm() { asm volatile("s_waitcnt vmcnt(%0)" :: "i"(N) : "memory"); }
template <int N> DI void wait_vm_bar() { asm volatile("s_waitcnt vmcnt(%0) lgkmcnt(0)\n\ts_barrier" :: "i"(N) : "memory"); }
DI void bar() { asm volatile("s_waitcnt lgkmcnt(0)\n\ts_barrier" ::: "memory"); }
template <class T> DI void pin(T& a) { asm volatile("" : "+v"(a)); }

template <int NP>
DI void poll_flags(const char* base, unsigned voff, bool lane_on, u64 tag) {
    const unsigned tl = (unsigned)tag, th = (unsigned)(tag >> 32);
    for (int spin = 0; spin < (1 << 20); ++spin) {
        u32x4 f0, f1;
        if (NP == 2)
            asm volatile("global_load_dwordx4 %0, %2, %3 sc1\n\tglobal_load_dwordx4 %1, %2, %3 offset:1024 sc1\n\ts_waitcnt vmcnt(0)"
                         : "=&v"(f0), "=&v"(f1) : "v"(voff), "s"(base) : "memory");
        else {
            asm volatile("global_load_dwordx4 %0, %1, %2 sc1\n\ts_waitcnt vmcnt(0)" : "=&v"(f0) : "v"(voff), "s"(base) : "memory");
            f1 = f0;
        }
        const bool ok = (f0.x == tl && f0.y == th && f0.z == tl && f0.w == th && f1.x == tl && f1.y == th && f1.z == tl && f1.w == th) || !lane_on;
        if (__all(ok)) break;
        __builtin_amdgcn_s_sleep(6);
    }
}

#define DIAG_DECL
#define STAMP(i)
#define DIAG_END

struct cf { float re, im; };
DI cf cmul(cf a, cf b) { cf r; r.re = a.re * b.re - a.im * b.im; r.im = a.re * b.im + a.im * b.re; return r; }
DI cf csq(cf a) { cf r; r.re = a.re * a.re - a.im * a.im; r.im = (a.re + a.re) * a.im; return r; }
DI cf zpow(float a2, float br, float l) {
    const float mag = __builtin_amdgcn_exp2f(a2 * l);
    const float fr = __builtin_amdgcn_fractf(br * l);
    cf r; r.re = mag * __builtin_amdgcn_cosf(fr); r.im = mag * __builtin_amdgcn_sinf(fr);
    return r;
}

struct KIdx { u16 v[2080]; };
constexpr KIdx make_kidx() {
    KIdx t{};
    for (int idx = 0; idx < 2080; ++idx) {
        if (idx >= 2048) { t.v[idx] = 768 * 4; continue; }
        const int m = 2047 - idx;
        int sh = 0;
        while ((m >> sh) >= 256) ++sh;
        int l = m;
        if (sh > 0) {
            int q = m >> sh; const int rem = m & ((1 << sh) - 1), half = 1 << (sh - 1);
            if (rem > half || (rem == half && (q & 1))) ++q;
            l = q << sh;
        }
        const int hi7 = l >> 7;
        int row = 0;
        if (l < 256) row = 8 * hi7 + (l & 7);
        else if (l < 512) row = 8 + 4 * hi7 + ((l >> 1) & 3);
        else if (l < 1024) row = 16 + 2 * hi7 + ((l >> 2) & 1);
        else row = 24 + hi7;
        t.v[idx] = (u16)((row * 16 + ((l >> 3) & 15)) * 4);
    }
    return t;
}
__device__ const KIdx g_kidx = make_kidx();

template <int TG, int OFF>
DI void conv_load(LDS char* pa, LDS char* pb, bf16x8 (&A)[8], bf16x8 (&B)[8]) {
    constexpr int NU = TG == 0 ? 8 : 7;
#pragma unroll
    for (int u = 0; u < NU; ++u) {
        A[u] = as_frag(*(LDS u32x4*)(pa + 512 * (7 - u) + OFF));
        B[u] = as_frag(*(LDS u32x4*)(pb + 512 * u + OFF));
    }
}
template <int TG>
DI void conv_mma(const bf16x8 (&A)[8], const bf16x8 (&B)[8], f32x4 (&acc)[4]) {
    constexpr int T0 = TG == 0 ? 0 : 1, T1 = TG == 0 ? 3 : 2, T2 = TG == 0 ? 4 : 5, T3 = TG == 0 ? 7 : 6;
#pragma unroll
    for (int u = 0; u <= T3; ++u) {
        acc[3] = mfma16(A[u], B[T3 - u], acc[3]);
        if (u <= T2) acc[2] = mfma16(A[u], B[T2 - u], acc[2]);
        if (u <= T1) acc[1] = mfma16(A[u], B[T1 - u], acc[1]);
        if (u <= T0) acc[0] = mfma16(A[u], B[T0 - u], acc[0]);
    }
}
DI bool flags_ok(const u32x4& f, u64 tag, bool lane_on) {
    const unsigned tl = (unsigned)tag, th = (unsigned)(tag >> 32);
    return (f.x == tl && f.y == th && f.z == tl && f.w == th) || !lane_on;
}
template <int TG>
DI void conv_run(LDS char* pa, LDS char* pb, f32x4 (&acc)[4]) {
    bf16x8 A0[8], B0[8], A1[8], B1[8];
    conv_load<TG, 192>(pa, pb, A0, B0);
    conv_load<TG, 128>(pa, pb, A1, B1); SB0();
    conv_mma<TG>(A0, B0, acc); SB0();
    conv_load<TG, 64>(pa, pb, A0, B0); SB0();
    conv_mma<TG>(A1, B1, acc); SB0();
    conv_load<TG, 0>(pa, pb, A1, B1); SB0();
    conv_mma<TG>(A0, B0, acc); SB0();
    conv_mma<TG>(A1, B1, acc);
}

constexpr int G1_PRE = 8;
struct G1Sched { int op[80]; int at[80]; int n; };
constexpr G1Sched make_g1() {
    G1Sched s{}; int n = 0;
    const int first[8] = {0, 1, 108, 109, 2, 3, 110, 111};
    for (int i = 0; i < 8; ++i) { s.op[n] = first[i]; s.at[n] = -1; ++n; }
    const int loop[13][2] = {{4, 112}, {113, 5}, {114, 115}, {6, 116}, {117, 7}, {118, 119}, {8, 120}, {121, 9}, {122, 123},
                             {10, 124}, {125, 11}, {126, 127}, {12, 13}};
    for (int t = 0; t < 13; ++t) for (int i = 0; i < 2; ++i) { s.op[n] = loop[t][i]; s.at[n] = t; ++n; }
    for (int t = 14; t < 28; ++t) { s.op[n] = 200 + (t - 14); s.at[n] = t; ++n; }
    s.n = n;
    return s;
}
constexpr G1Sched G1S = make_g1();
constexpr int g1_need_x(int st) { return st == -1 ? 0 : st == 1 ? 3 : st == 7 ? 5 : st == 11 ? 7 : st == 15 ? 9 : st == 19 ? 11 : st == 23 ? 13 : -1; }
constexpr int g1_need_w(int st) { return st == -1 ? 1 : st == 1 ? 7 : st == 7 ? 11 : st == 11 ? 15 : st == 15 ? 19 : st == 19 ? 23 : st == 23 ? 27 : -1; }
constexpr int g1_wait(int st) {
    const int lim = st < 0 ? 0 : st;
    int issued = 0, last = -1;
    for (int i = 0; i < G1S.n; ++i) {
        if (G1S.at[i] >= lim) break;
        const int o = G1S.op[i];
        if ((o < 100 && o <= g1_need_x(st)) || (o >= 100 && o < 200 && o - 100 <= g1_need_w(st))) last = issued;
        ++issued;
    }
    return issued - 1 - last;
}
static_assert(g1_wait(-1) == 7 && g1_wait(1) == 4 && g1_wait(7) == 10 && g1_wait(11) == 12 && g1_wait(15) == 11 && g1_wait(19) == 9 && g1_wait(23) == 9, "gemm1 schedule");
struct G1Ctx { unsigned voffA; const char* baseA; unsigned ldsA; unsigned voffW; const char* wE; const char* wO; const char* wD; };
template <int I, int AT>
DI void g1_issue(u32x4 (&wf)[28], const G1Ctx& c) {
    if constexpr (I < G1S.n) {
        if constexpr (G1S.at[I] == AT) {
            constexpr int o = G1S.op[I];
            if constexpr (o < 100) glds16_c(c.voffA, c.baseA + (size_t)(32 * o) * (L_ * 2), c.ldsA + 32 * o * 256);
            else if constexpr (o < 200) ld16_c<((o - 100) & 3) * 1024>(wf[o - 100], c.voffW, (((o - 100) & 1) ? c.wO : c.wE) + ((o - 100) >> 2) * 4096);
            else ld16_c<((o - 200) & 3) * 1024>(wf[o - 200], c.voffW, c.wD + ((o - 200) >> 2) * 4096);
        }
        g1_issue<I + 1, AT>(wf, c);
    }
}
template <int K0, int K1>
DI void pin_range(u32x4 (&wf)[28]) { if constexpr (K0 <= K1) { pin(wf[K0]); pin_range<K0 + 1, K1>(wf); } }
constexpr int g1_prev_w(int st) { return st == 1 ? 7 : st == 7 ? 7 : st == 11 ? 11 : st == 15 ? 15 : st == 19 ? 19 : st == 23 ? 23 : -1; }
template <int S>
DI void g1_steps(u32x4 (&wf)[28], f32x16 (&acc)[4], bf16x8 (&cur)[4], bf16x8 (&nxt)[4], LDS char* (&pE)[4], LDS char* (&pO)[4],
                 float (&st)[4], const G1Ctx& c) {
    if constexpr (S < 28) {
        if constexpr (g1_need_x(S) >= 0) { wait_vm_bar<g1_wait(S)>(); pin_range<g1_prev_w(S) + 1, g1_need_w(S)>(wf); }
        const u32x4 w = wf[S];
        const u32x4 xu = __builtin_bit_cast(u32x4, cur[0]);
#pragma unroll
        for (int t = 0; t < 4; ++t) {
            acc[t] = mfma32(as_frag(w), cur[t], acc[t]);
            if constexpr (S + 1 < 28) {
                LDS char* p = (((S + 1) & 1) ? pO[t] : pE[t]) + (S + 1) * 4096;
                nxt[t] = tr_frag(p, p + 1024);
            }
            if constexpr ((S & 1) == 0) {
                const unsigned xv_ = xu[t];
                const float lo = bflo(xv_), hi = bfhi(xv_);
                st[0] += lo; st[1] += hi; st[2] = fmaf(lo, lo, st[2]); st[3] = fmaf(hi, hi, st[3]);
            }
            SB0();
            if (t == 0) { g1_issue<0, S>(wf, c); SB0(); }
        }
        g1_steps<S + 1>(wf, acc, nxt, cur, pE, pO, st, c);
    }
}

constexpr int G2_AHEAD = 7, G2_RING = 8;
template <int C>
DI void g2_issue(unsigned voff, const char* baseH, unsigned ldsH) {
    glds16_c(voff, baseH + (size_t)(2 * C) * 1024, ldsH + (C % G2_RING) * 16384);
    glds16_c(voff, baseH + (size_t)(2 * C + 1) * 1024, ldsH + (C % G2_RING) * 16384 + 1024);
}
template <int C>
DI void g2_prologue(unsigned voff, const char* baseH, unsigned ldsH) {
    if constexpr (C < G2_AHEAD) { g2_issue<C>(voff, baseH, ldsH); g2_prologue<C + 1>(voff, baseH, ldsH); }
}
template <int C>
DI void g2_read(u32x4 (&hb)[4], LDS char* p) {
    LDS char* q = p + (C % G2_RING) * 16384;
    hb[0] = *(LDS u32x4*)(q); hb[1] = *(LDS u32x4*)(q + 1024);
    hb[2] = *(LDS u32x4*)(q + 2048); hb[3] = *(LDS u32x4*)(q + 3072);
}
template <int C>
DI void g2_chunks(u32x4 (&wf)[28], f32x16 (&acc)[2], u32x4 (&ha)[4], u32x4 (&hb)[4], LDS char* prd,
                  unsigned voff, const char* baseH, unsigned ldsH) {
    if constexpr (C < 14) {
        if constexpr (C + 1 < 14) {
            constexpr int issued = (C + G2_AHEAD < 14) ? C + G2_AHEAD : 14;
            wait_vm_bar<2 * (issued - (C + 2))>();
            if constexpr (C + G2_AHEAD < 14) g2_issue<C + G2_AHEAD>(voff, baseH, ldsH);
            g2_read<C + 1>(hb, prd); SB0();
        }
        acc[0] = mfma32(as_frag(wf[2 * C]), as_frag(ha[0]), acc[0]);
        acc[1] = mfma32(as_frag(wf[2 * C]), as_frag(ha[2]), acc[1]);
        acc[0] = mfma32(as_frag(wf[2 * C + 1]), as_frag(ha[1]), acc[0]);
        acc[1] = mfma32(as_frag(wf[2 * C + 1]), as_frag(ha[3]), acc[1]);
        SB0();
        g2_chunks<C + 1>(wf, acc, hb, ha, prd, voff, baseH, ldsH);
    }
}
template <int S, int E>
DI void load_wf(u32x4 (&wf)[28], unsigned voff, const char* wE, const char* wO) {
    if constexpr (S < E) {
        ld16_c<(S & 3) * 1024>(wf[S], voff, ((S & 1) ? wO : wE) + (S >> 2) * 4096);
        load_wf<S + 1, E>(wf, voff, wE, wO);
    }
}

__global__ __launch_bounds__(512, 2) void fused_block(
    const u16* __restrict__ x, const u16* __restrict__ Are, const u16* __restrict__ Aim,
    const u16* __restrict__ Bre, const u16* __restrict__ Bim, const u16* __restrict__ Cre,
    const u16* __restrict__ Cim, const u16* __restrict__ logdt, const u16* __restrict__ dskip,
    const u16* __restrict__ wg, const u16* __restrict__ wu, const u16* __restrict__ wd,
    const u16* __restrict__ cvec, const u16* __restrict__ wmod, char* __restrict__ ws, u16* __restrict__ out)
{
    DIAG_DECL STAMP(0);
    extern __shared__ __attribute__((aligned(16))) char smem[];
    LDS char* lds = (LDS char*)smem;
    const unsigned ldsb = (unsigned)(size_t)lds;
    const int tid = threadIdx.x, lane = tid & 63;
    const int wid = __builtin_amdgcn_readfirstlane(tid >> 6);
    const int b = blockIdx.x, xc = b & 7, q = b >> 3;
    const bool mainb = q < 28;
    const int j = xc * 28 + q;
    const int me = xc * 32 + q;
    const bool doB = q < 21;
    const int gB = xc * 21 + (doB ? q : q - 21), cgB = gB >> 2, kqB = gB & 3;

    u32x2 epv;
    ld8_c(epv, 0u, ws + WS_EP);
    const int ch = wid >> 2, n = tid & 255, dch = mainb ? 2 * j + ch : ch;
    unsigned pAre, pAim, pBre, pBim, pCre, pCim, pdt;
    {
        const unsigned o = (unsigned)(dch * NM + n) * 2;
        ld2(pAre, o, Are); ld2(pAim, o, Aim); ld2(pBre, o, Bre); ld2(pBim, o, Bim); ld2(pCre, o, Cre); ld2(pCim, o, Cim);
        ld2(pdt, 0u, logdt + dch);
    }
    u32x4 xv;
    ld16(xv, (unsigned)(lane < 56 ? lane : 55) * 16, x + (size_t)(8 * me + wid) * D_);
    u32x4 w0, w1; unsigned c0, c1;
    const int kB0 = tid >> 3, kB1 = kB0 + 64 < 112 ? kB0 + 64 : kB0;
    {
        const u16* wb = wmod + (size_t)(112 * kqB) * 2688 + 64 * cgB;
        ld2(c0, kB0 * 2, cvec + 112 * kqB); ld2(c1, kB1 * 2, cvec + 112 * kqB);
        ld16(w0, (unsigned)(kB0 * 2688 + 8 * (tid & 7)) * 2, wb); ld16(w1, (unsigned)(kB1 * 2688 + 8 * (tid & 7)) * 2, wb);
    }
    u32x4 wdv; unsigned mdk;
    {
        const int t2 = tid < 448 ? tid : 447, row = t2 / 28, cc = t2 - 28 * row, jj = mainb ? j : 0;
        ld16(wdv, (unsigned)((16 * (jj >> 1) + row) * D_ + 224 * (jj & 1) + 8 * cc) * 2, wd);
        ld4(mdk, 0u, dskip + 2 * jj);
    }
    u32x4 kx0, kx1;
    ld16(kx0, n * 16, g_kidx.v); ld16<16>(kx1, n * 16, g_kidx.v);
    STAMP(1);
    wait_vm<8>();
    STAMP(25);
    pin(xv); pin(epv);
    const u64 tag = (((u64)epv.y << 32) | epv.x) + 1;
    const u32x2 tagv = {(unsigned)tag, (unsigned)(tag >> 32)};
    {
        if (lane < 56) *(LDS u32x4*)(lds + O_WS + wid * 896 + lane * 16) = xv;
        float v[8] = {bflo(xv.x), bfhi(xv.x), bflo(xv.y), bfhi(xv.y), bflo(xv.z), bfhi(xv.z), bflo(xv.w), bfhi(xv.w)};
        float s = ((v[0] + v[1]) + (v[2] + v[3])) + ((v[4] + v[5]) + (v[6] + v[7]));
        float qq = 0.f;
#pragma unroll
        for (int i = 0; i < 8; ++i) qq = fmaf(v[i], v[i], qq);
        if (lane >= 56) { s = 0.f; qq = 0.f; }
        const float mean = wave_sum(s) * (1.f / D_);
        float q2 = 0.f;
#pragma unroll
        for (int i = 0; i < 8; ++i) { const float t = v[i] - mean; q2 = fmaf(t, t, q2); }
        if (lane >= 56) q2 = 0.f;
        const float var = wave_sum(q2) * (1.f / D_);
        const float sd = rbf(sqrtf(rbf(rbf(var) + EPS)));
        if (lane == 0) *(LDS unsigned*)(lds + O_SB + wid * 4) = pk2(mean, sd);
    }
    STAMP(26);
    wait_vm<2>();
    STAMP(27);
    pin(c0); pin(c1); pin(w0); pin(w1); pin(wdv); pin(mdk);
    if (tid < 448) *(LDS u32x4*)(lds + O_WS + 7168 + tid * 16) = wdv;
    if (doB) {
        const float f0 = bf2f(c0), f1 = bf2f(c1);
        const float s0 = rbf(f0 / (1.f + __expf(-f0)));
        const float s1 = kB0 + 64 < 112 ? rbf(f1 / (1.f + __expf(-f1))) : 0.f;
        float p[8];
        p[0] = s0 * bflo(w0.x) + s1 * bflo(w1.x); p[1] = s0 * bfhi(w0.x) + s1 * bfhi(w1.x);
        p[2] = s0 * bflo(w0.y) + s1 * bflo(w1.y); p[3] = s0 * bfhi(w0.y) + s1 * bfhi(w1.y);
        p[4] = s0 * bflo(w0.z) + s1 * bflo(w1.z); p[5] = s0 * bfhi(w0.z) + s1 * bfhi(w1.z);
        p[6] = s0 * bflo(w0.w) + s1 * bflo(w1.w); p[7] = s0 * bfhi(w0.w) + s1 * bfhi(w1.w);
        const float a0 = swap32_add(p[0], p[4]), a1 = swap32_add(p[1], p[5]), a2 = swap32_add(p[2], p[6]), a3 = swap32_add(p[3], p[7]);
        const float b0 = swap16_add(a0, a2), b1 = swap16_add(a1, a3);
        const bool up8 = (lane & 8) != 0;
        float r = up8 ? b1 : b0;
        const float oth = up8 ? b0 : b1;
        r += __int_as_float(__builtin_amdgcn_update_dpp(0, __float_as_int(oth), 0x128, 0xf, 0xf, true));
        const int col = 8 * (lane & 7) + 4 * (lane >> 5) + 2 * ((lane >> 4) & 1) + ((lane >> 3) & 1);
        *(LDS float*)(lds + O_MR + (wid * 64 + col) * 4) = r;
    }
    bar();
    if (wid == 0) {
        if (lane < 2) st16_c((8 * me + 4 * lane) * 4, ws + WS_STATS, *(LDS u32x4*)(lds + O_SB + lane * 16));
        if (doB) {
            float t = 0.f;
#pragma unroll
            for (int w = 0; w < 8; ++w) t += *(LDS float*)(lds + O_MR + (w * 64 + lane) * 4);
            st4_c((kqB * 2688 + 64 * cgB + lane) * 4, ws + WS_MOD, __float_as_uint(t));
        }
    }
    if (tid >= 256 && tid < 480) {
        const int pr = tid - 256;
        unsigned e[8];
#pragma unroll
        for (int i = 0; i < 8; ++i) e[i] = *(LDS unsigned*)(lds + O_WS + i * 896 + pr * 4);
        st16_c((unsigned)pr * (L_ * 4) + me * 32, ws + WS_XT, u32x4{e[0], e[1], e[2], e[3]});
        st16_c((unsigned)pr * (L_ * 4) + me * 32 + 16, ws + WS_XT, u32x4{e[4], e[5], e[6], e[7]});
    }
    if (mainb && wid < 7) {
        const int h = lane >> 5, blk = (lane >> 4) & 1, qq = (lane & 15) >> 2, p = lane & 3;
        LDS char* a0 = lds + O_WS + 7168 + (4 * h + qq) * 448 + (32 * wid + 16 * blk + 4 * p) * 2;
        const u32x4 w = __builtin_bit_cast(u32x4, tr_frag(a0, a0 + 8 * 448));
        st16_c(lane * 16, ws + WS_WDF + (size_t)((7 * (j & 1) + wid) * 112 + (j >> 1)) * 1024, w);
    }
    STAMP(2);

    if (!mainb) {
        const int sp = xc * 4 + (q - 28);
        wait_vm_bar<0>();
        if (wid == 0 && lane < 8) st8_c(lane * 2048 + me * 8, ws + WS_FA, tagv);
        if (sp < 28) {
            STAMP(33);
            {
                const unsigned vo = (unsigned)((lane >> 3) * F_ + 8 * (lane & 7)) * 2;
#pragma unroll
                for (int r = 0; r < 14; ++r) {
                    const int i8 = wid + 8 * r;
                    const int mat = i8 >= 56 ? 1 : 0, row0 = 8 * (i8 - 56 * mat);
                    glds16(vo, (mat ? wu : wg) + (size_t)row0 * F_ + 64 * sp, ldsb + i8 * 1024);
                }
            }
            STAMP(34);
            if (wid == 0) poll_flags<2>(ws + WS_FA + xc * 2048, lane * 16, true, tag);
            bar();
            STAMP(35);
            {
                unsigned mc[4], ms[4];
                const int t2 = tid < 448 ? tid : 447;
#pragma unroll
                for (int k = 0; k < 4; ++k) { ld4_c(mc[k], (unsigned)(k * 2688 + 4 * 448 + t2) * 4, ws + WS_MOD); ld4_c(ms[k], (unsigned)(k * 2688 + 3 * 448 + t2) * 4, ws + WS_MOD); }
                wait_vm<0>();
#pragma unroll
                for (int k = 0; k < 4; ++k) { pin(mc[k]); pin(ms[k]); }
                const float cbv = rbf((__uint_as_float(mc[0]) + __uint_as_float(mc[1])) + (__uint_as_float(mc[2]) + __uint_as_float(mc[3])));
                const float sbv = (__uint_as_float(ms[0]) + __uint_as_float(ms[1])) + (__uint_as_float(ms[2]) + __uint_as_float(ms[3]));
                const unsigned pr = pk2(1.f + cbv, sbv);
                if (tid < 448) { *(LDS u16*)(lds + 114688 + tid * 2) = (u16)pr; *(LDS u16*)(lds + 114688 + 896 + tid * 2) = (u16)(pr >> 16); }
                else *(LDS unsigned*)(lds + 114688 + 1792 + (tid - 448) * 4) = 0u;
            }
            bar();
            {
                const int i = lane & 15, g = lane >> 4, mat = wid >> 2, cg = wid & 3;
                LDS char* pa = lds + 114688 + (i == 0 ? 0 : i == 1 ? 896 : 1792);
                const int astep = i < 2 ? 64 : 0, aoff = i < 2 ? g * 16 : g * 16;
                LDS char* pb = lds + mat * 57344 + (8 * g + ((lane & 15) >> 2)) * 128 + cg * 32 + 8 * (lane & 3);
                f32x4 acc = {0.f, 0.f, 0.f, 0.f};
#pragma unroll
                for (int s = 0; s < 14; ++s)
                    acc = mfma16(as_frag(*(LDS u32x4*)(pa + aoff + s * astep)), tr_frag(pb + s * 4096, pb + s * 4096 + 512), acc);
                if (lane < 16) {
                    const int f = 64 * sp + 16 * cg + lane;
                    const unsigned o = (unsigned)((f >> 4) * 32 + mat * 16 + (f & 15)) * 4;
                    st4_c(o, ws + WS_UB, __float_as_uint(acc[0]));
                    st4_c(o + 3584 * 4, ws + WS_UB, __float_as_uint(acc[1]));
                }
            }
        }
        wait_vm_bar<0>();
        if (wid == 0 && lane < 8) st8_c(lane * 2048 + me * 8, ws + WS_FC, tagv);
        DIAG_END;
        return;
    }

    pin(pAre); pin(pAim); pin(pBre); pin(pBim); pin(pCre); pin(pCim); pin(pdt);
    u32x4 wr[2];
    {
        const float dt = rbf(__expf(bf2f(pdt)));
        const float a = dt * bf2f(pAre), bb = dt * bf2f(pAim);
        const float dBr = dt * bf2f(pBre), dBi = dt * bf2f(pBim);
        const float fCr = bf2f(pCre), fCi = bf2f(pCim);
        cf W; W.re = fCr * dBr - fCi * dBi; W.im = fCr * dBi + fCi * dBr;
        const float a2 = a * 1.4426950408889634f, br = bb * 0.15915494309189535f;
        LDS char* tb = lds + ch * T_CH + n * 4;
#define PUT(row, v) (*(LDS unsigned*)(tb + (row) * T_ROW) = pk2((v).re, (v).im))
#define PUTC(row, v) (*(LDS unsigned*)(tb + (row) * T_ROW) = pk2((v).re, -(v).im))
        const cf z1 = zpow(a2, br, 1.f), z8 = zpow(a2, br, 8.f), z128 = zpow(a2, br, 128.f);
        {
            cf q0; q0.re = 1.f; q0.im = 0.f;
            const cf q1 = z8, q2 = csq(q1), q3 = cmul(q2, q1), q4 = csq(q2), q5 = cmul(q4, q1), q6 = csq(q3), q7 = cmul(q4, q3);
            const cf q8 = csq(q4);
            PUTC(41, q0); PUTC(42, q1); PUTC(43, q2); PUTC(44, q3); PUTC(45, q4); PUTC(46, q5); PUTC(47, q6); PUTC(48, q7);
            PUTC(49, q8);
            cf t;
            t = cmul(q8, q1); PUTC(50, t); t = csq(q5); PUTC(51, t); t = cmul(q8, q3); PUTC(52, t);
            t = csq(q6); PUTC(53, t); t = cmul(q8, q5); PUTC(54, t); t = csq(q7); PUTC(55, t); t = cmul(q8, q7); PUTC(56, t);
        }
        SB0();
        {
            const int ks = j >> 3, cgp = j & 7;
#pragma unroll
            for (int r = 0; r < 2; ++r) {
                const int c = (tid + 512 * r < 896) ? tid + 512 * r : 895;
                const int row = c / 56, cc = c - 56 * row, mat = cc >= 28 ? 1 : 0;
                ld16v(wr[r], (mat ? wu : wg) + (size_t)(16 * ks + row) * F_ + 224 * cgp + 8 * (cc - 28 * mat));
            }
        }
        SB0();
        {
            const cf z2 = csq(z1), z4 = csq(z2);
            const cf z256 = csq(z128), z512 = csq(z256), z1024 = csq(z512);
            cf P[16];
            P[0] = W; P[1] = cmul(W, z1); P[2] = cmul(W, z2); P[3] = cmul(P[2], z1);
            P[4] = cmul(W, z4); P[5] = cmul(P[4], z1); P[6] = cmul(P[4], z2); P[7] = cmul(P[6], z1);
#pragma unroll
            for (int c = 0; c < 8; ++c) P[8 + c] = cmul(P[c], z128);
#pragma unroll
            for (int c = 0; c < 16; ++c) PUT(c, P[c]);
            cf S1[8];
#pragma unroll
            for (int i = 0; i < 8; ++i) { S1[i] = cmul(P[8 * (i >> 2) + 2 * (i & 3)], z256); PUT(16 + i, S1[i]); }
            cf S2[8];
#pragma unroll
            for (int i = 0; i < 8; ++i) {
                const int a_ = i >> 1, c_ = i & 1;
                S2[i] = (a_ < 2) ? cmul(P[8 * a_ + 4 * c_], z512) : cmul(S1[4 * (a_ - 2) + 2 * c_], z512);
                PUT(24 + i, S2[i]);
            }
            cf S30;
#pragma unroll
            for (int a_ = 0; a_ < 8; ++a_) {
                const cf src = (a_ < 2) ? P[8 * a_] : (a_ < 4) ? S1[4 * (a_ - 2)] : S2[2 * (a_ - 4)];
                const cf t = cmul(src, z1024);
                if (a_ == 0) S30 = t;
                PUT(32 + a_, t);
            }
            const cf t = cmul(S30, z1024);
            PUT(40, t);
        }
#undef PUT
#undef PUTC
    }
    if (tid < 2) *(LDS float*)(lds + O_KC + tid * KC_CH + 768 * 4) = 0.f;
    STAMP(3);
    wait_vm_bar<0>();
    pin(wr[0]); pin(wr[1]);
    if (wid == 0 && lane < 8) st8_c(lane * 2048 + me * 8, ws + WS_FA, tagv);
    STAMP(4);
    const int mi = 4 * (xc >> 1) + (q & 3), ni = 7 * (xc & 1) + (q >> 2);
    const int mi2 = 2 * mi + (xc & 1), ni2 = q >> 2;
    STAMP(23);
    if (wid < 6) {
        const int c2 = wid >= 3 ? 1 : 0, tile = wid - 3 * c2;
        LDS char* tb = lds + c2 * T_CH;
        LDS char* pa = tb + (tile * 16 + (lane & 15)) * T_ROW + (lane >> 4) * 16;
        LDS char* pb = tb + (41 + (lane & 15)) * T_ROW + (lane >> 4) * 16;
        f32x4 acc0 = {0.f, 0.f, 0.f, 0.f}, acc1 = {0.f, 0.f, 0.f, 0.f};
#pragma unroll
        for (int s = 0; s < 16; s += 2) {
            acc0 = mfma16(as_frag(*(LDS u32x4*)(pa + s * 64)), as_frag(*(LDS u32x4*)(pb + s * 64)), acc0);
            acc1 = mfma16(as_frag(*(LDS u32x4*)(pa + s * 64 + 64)), as_frag(*(LDS u32x4*)(pb + s * 64 + 64)), acc1);
        }
        acc0 += acc1;
        LDS float* kc = (LDS float*)(lds + O_KC + c2 * KC_CH) + (tile * 16 + 4 * (lane >> 4)) * 16 + (lane & 15);
#pragma unroll
        for (int e = 0; e < 4; ++e) kc[e * 16] = acc0[e];
    } else if (wid == 7) {
        __builtin_amdgcn_s_sleep(12);
        poll_flags<2>(ws + WS_FA + xc * 2048, lane * 16, true, tag);
    }
    STAMP(24);
    bar();
    STAMP(5);
    u32x4 stq, xq; u32x2 mp; unsigned mq[4] = {0u, 0u, 0u, 0u};
    {
        ld16_c(xq, tid * 16, ws + WS_XT + (size_t)j * (L_ * 4));
        ld16_c(stq, tid * 16, ws + WS_STATS);
        const int l12 = lane < 12 ? lane : 0;
        ld8_c(mp, (unsigned)((l12 & 3) * 2688 + (l12 >> 2) * 448 + 2 * j) * 4, ws + WS_MOD);
    }
    {
        const int mo = wid == 7 ? 5 * 448 + 64 * ni2 + lane : 4 * 448 + 16 * (j >> 3) + (lane & 15);
        if (wid == 0 || wid == 7) {
#pragma unroll
            for (int k = 0; k < 4; ++k) ld4_c_io(mq[k], (unsigned)(k * 2688 + mo) * 4, ws + WS_MOD);
        }
    }
    {
        pin(kx0); pin(kx1);
        LDS char* kc = lds + O_KC + ch * KC_CH;
        const unsigned ix[8] = {kx0.x, kx0.y, kx0.z, kx0.w, kx1.x, kx1.y, kx1.z, kx1.w};
        unsigned d[8], a[7];
#pragma unroll
        for (int e = 0; e < 8; ++e)
            d[e] = pk2(*(LDS float*)(kc + (ix[e] & 0xffffu)), *(LDS float*)(kc + (ix[e] >> 16)));
#pragma unroll
        for (int e = 0; e < 7; ++e) a[e] = __builtin_amdgcn_alignbit(d[e + 1], d[e], 16);
        LDS char* rc = lds + ch * RCH + 16 * n;
#pragma unroll
        for (int c = 0; c < 4; ++c) {
            *(LDS u32x4*)(rc + (2 * c) * RC) = u32x4{d[c], d[c + 1], d[c + 2], d[c + 3]};
            *(LDS u32x4*)(rc + (2 * c + 1) * RC) = u32x4{a[c], a[c + 1], a[c + 2], a[c + 3]};
        }
        if (n < 8) *(LDS u32x4*)(lds + ch * RCH + n * RC + 16 * 256) = u32x4{0, 0, 0, 0};
        if (n >= 32 && n < 64) *(LDS u32x4*)(lds + O_H + ch * HCH + 16 * (n - 32)) = u32x4{0, 0, 0, 0};
    }
    STAMP(6);
    *(LDS u32x4*)(lds + O_WS + tid * 16) = wr[0];
    if (tid < 384) *(LDS u32x4*)(lds + O_WS + (tid + 512) * 16) = wr[1];
    wait_vm<0>();
    pin(stq); pin(mp); pin(mq[0]); pin(mq[1]); pin(mq[2]); pin(mq[3]); pin(xq);
    STAMP(7);
    float sa0, sa1, ca0_, ca1_, ga0, ga1;
    {
        float m0 = __uint_as_float(mp.x), m1 = __uint_as_float(mp.y);
        m0 = dpp_add<0xb1, 0xf>(m0); m1 = dpp_add<0xb1, 0xf>(m1);
        m0 = dpp_add<0x4e, 0xf>(m0); m1 = dpp_add<0x4e, 0xf>(m1);
        m0 = rbf(m0); m1 = rbf(m1);
        sa0 = __int_as_float(__builtin_amdgcn_readlane(__float_as_int(m0), 0)); sa1 = __int_as_float(__builtin_amdgcn_readlane(__float_as_int(m1), 0));
        ca0_ = __int_as_float(__builtin_amdgcn_readlane(__float_as_int(m0), 4)); ca1_ = __int_as_float(__builtin_amdgcn_readlane(__float_as_int(m1), 4));
        ga0 = __int_as_float(__builtin_amdgcn_readlane(__float_as_int(m0), 8)); ga1 = __int_as_float(__builtin_amdgcn_readlane(__float_as_int(m1), 8));
    }
    {
        const float m[4] = {bflo(stq.x), bflo(stq.y), bflo(stq.z), bflo(stq.w)};
        const float sd[4] = {bfhi(stq.x), bfhi(stq.y), bfhi(stq.z), bfhi(stq.w)};
        const float c10 = rbf(1.f + ca0_), c11 = rbf(1.f + ca1_);
        const float x0[4] = {bflo(xq.x), bflo(xq.y), bflo(xq.z), bflo(xq.w)};
        const float x1[4] = {bfhi(xq.x), bfhi(xq.y), bfhi(xq.z), bfhi(xq.w)};
        float h0[4], h1[4];
#pragma unroll
        for (int e = 0; e < 4; ++e) {
            const float r = __builtin_amdgcn_rcpf(sd[e]);
            h0[e] = (x0[e] - m[e]) * (r * c10) + sa0;
            h1[e] = (x1[e] - m[e]) * (r * c11) + sa1;
        }
        *(LDS u32x2*)(lds + O_H + 512 + 8 * tid) = u32x2{pk2(h0[0], h0[1]), pk2(h0[2], h0[3])};
        *(LDS u32x2*)(lds + O_H + HCH + 512 + 8 * tid) = u32x2{pk2(h1[0], h1[1]), pk2(h1[2], h1[3])};
        *(LDS u32x2*)(lds + O_XR + 8 * tid) = u32x2{__builtin_amdgcn_perm(xq.y, xq.x, 0x05040100u), __builtin_amdgcn_perm(xq.w, xq.z, 0x05040100u)};
        *(LDS u32x2*)(lds + O_XR + 4096 + 8 * tid) = u32x2{__builtin_amdgcn_perm(xq.y, xq.x, 0x07060302u), __builtin_amdgcn_perm(xq.w, xq.z, 0x07060302u)};
        const float mv = rbf((__uint_as_float(mq[0]) + __uint_as_float(mq[1])) + (__uint_as_float(mq[2]) + __uint_as_float(mq[3])));
        if (tid < 16) *(LDS float*)(lds + O_MD + tid * 4) = rbf(1.f + mv);
        else if (wid == 7) *(LDS float*)(lds + O_GB + lane * 4) = mv;
    }
    bar();
    STAMP(8);
    {
        const int h = lane >> 5, blk = (lane >> 4) & 1, qq = (lane & 15) >> 2, p = lane & 3;
        const f32x4 ca0 = *(LDS f32x4*)(lds + O_MD + (8 * h) * 4);
        const f32x4 ca1 = *(LDS f32x4*)(lds + O_MD + (8 * h + 4) * 4);
#pragma unroll
        for (int r = 0; r < 2; ++r) {
            const int fgl = wid + 8 * r;
            if (fgl < 14) {
                LDS char* a0 = lds + O_WS + (8 * h + qq) * 896 + blk * 448 + fgl * 32 + 8 * p;
                const u32x4 w = __builtin_bit_cast(u32x4, tr_frag(a0, a0 + 4 * 896));
                u32x4 o;
                o.x = pk2(bflo(w.x) * ca0[0], bfhi(w.x) * ca0[1]); o.y = pk2(bflo(w.y) * ca0[2], bfhi(w.y) * ca0[3]);
                o.z = pk2(bflo(w.z) * ca1[0], bfhi(w.z) * ca1[1]); o.w = pk2(bflo(w.w) * ca1[2], bfhi(w.w) * ca1[3]);
                st16_c(lane * 16, ws + WS_WPF + (size_t)((14 * (j & 7) + fgl) * 28 + (j >> 3)) * 1024, o);
            }
        }
    }
    STAMP(9);
    const int wch = wid & 1, tg = (wid >> 1) & 1, pg = wid >> 2;
    const int bb = lane & 15, g = lane >> 4;
    f32x4 cacc[4] = {};
    {
        LDS char* pa0 = lds + wch * RCH + (7 - (bb & 7)) * RC
                        + 2 * (2040 - 16 * (g >> 1) + 8 * (g & 1) - 8 * (bb >> 3) - 256 * 7) - 256 * pg - 192;
        LDS char* pb0 = lds + O_H + wch * HCH + 2 * (256 + 16 * bb - 16 * (g >> 1) + 8 * (g & 1)) - 256 * pg - 192;
        if (tg == 0) conv_run<0>(pa0, pb0, cacc);
        else conv_run<1>(pa0, pb0, cacc);
    }
    STAMP(10);
    {
        LDS char* mine = lds + O_RED + wid * 2048 + lane * 16;
        LDS char* theirs = lds + O_RED + (wid ^ 4) * 2048 + lane * 16;
        *(LDS f32x4*)(mine) = pg ? cacc[0] : cacc[2];
        *(LDS f32x4*)(mine + 1024) = pg ? cacc[1] : cacc[3];
        wait_vm_bar<0>();
        if (wid == 0 && lane < 8) st8_c(lane * 2048 + me * 8, ws + WS_FB, tagv);
        const f32x4 o0 = *(LDS f32x4*)(theirs), o1 = *(LDS f32x4*)(theirs + 1024);
        const f32x4 y0 = (pg ? cacc[2] : cacc[0]) + o0, y1 = (pg ? cacc[3] : cacc[1]) + o1;
        const int dd = 2 * j + wch;
        const float ga = wch ? ga1 : ga0, dsk = wch ? bfhi(mdk) : bflo(mdk);
#pragma unroll
        for (int k = 0; k < 2; ++k) {
            const int idx = 2 * pg + k;
            const int t = tg == 0 ? (idx == 0 ? 0 : idx == 1 ? 3 : idx == 2 ? 4 : 7) : (idx == 0 ? 1 : idx == 1 ? 2 : idx == 2 ? 5 : 6);
            const int l = 256 * t + 16 * bb + 4 * g;
            const f32x4 y = k ? y1 : y0;
            const u32x2 hv = *(LDS u32x2*)(lds + O_H + wch * HCH + 512 + 2 * l);
            const u32x2 xr = *(LDS u32x2*)(lds + O_XR + wch * 4096 + 2 * l);
            const float o0_ = bflo(xr.x) + ga * (y[0] + bflo(hv.x) * dsk), o1_ = bfhi(xr.x) + ga * (y[1] + bfhi(hv.x) * dsk);
            const float o2_ = bflo(xr.y) + ga * (y[2] + bflo(hv.y) * dsk), o3_ = bfhi(xr.y) + ga * (y[3] + bfhi(hv.y) * dsk);
            st8_c((unsigned)l * 2, ws + WS_X1T + (size_t)dd * (L_ * 2), u32x2{pk2(o0_, o1_), pk2(o2_, o3_)});
        }
    }
    const int wq = wid & 3, par = wid >> 2;
    const int dh = wid & 1, kq = wid >> 1;
    STAMP(11);
    wait_vm_bar<0>();
    STAMP(12);
    if (wid == 0) {
        if (lane < 8) st8_c(lane * 2048 + me * 8, ws + WS_FC, tagv);
        poll_flags<2>(ws + WS_FB + xc * 2048, lane * 16, (lane & 15) < 14, tag);
    }
    bar();
    STAMP(28);
    u32x4 wf[28];
    unsigned ub0;
    G1Ctx gc;
    {
        const char* wb = ws + WS_WPF + (size_t)(8 * ni + wid) * 28 * 1024;
        const int rr = lane >> 4, cc = (lane & 15) ^ (rr << 2);
        gc.voffA = (unsigned)(rr * L_ + 8 * cc) * 2;
        gc.baseA = ws + WS_X1T + ((size_t)(4 * wid) * L_ + 128 * mi) * 2;
        gc.ldsA = ldsb + 4 * wid * 256;
        gc.voffW = lane * 16; gc.wE = wb + par * 1024; gc.wO = wb - par * 1024;
        gc.wD = ws + WS_WDF + (size_t)((2 * ni2 + dh) * 112 + 28 * kq) * 1024;
        load_wf<0, G1_PRE>(wf, gc.voffW, gc.wE, gc.wO);
    }
    STAMP(13);
    if (wid == 0) poll_flags<2>(ws + WS_FC + xc * 2048, lane * 16, true, tag);
    bar();
    STAMP(14);
    f32x16 acc[4] = {};
    float st4[4] = {0.f, 0.f, 0.f, 0.f};
    {
        ld4_c(ub0, (tid & 255) * 4, ws + WS_UB + (size_t)(par * 3584 + ni * 256) * 4);
        g1_issue<0, -1>(wf, gc);
        const int h = lane >> 5, blk = (lane >> 4) & 1, qq = (lane & 15) >> 2, p = lane & 3;
        LDS char* pE[4]; LDS char* pO[4];
#pragma unroll
        for (int t = 0; t < 4; ++t) {
            LDS char* pb = lds + (8 * h + qq) * 256 + 64 * ((t ^ wq) ^ qq) + 32 * blk + 8 * p;
            pE[t] = pb + par * 4096; pO[t] = pb - par * 4096;
        }
        wait_vm_bar<g1_wait(-1)>();
        pin_range<0, G1_PRE - 1>(wf); pin(ub0);
        STAMP(15);
        bf16x8 xa[4], xb[4];
#pragma unroll
        for (int t = 0; t < 4; ++t) xa[t] = tr_frag(pE[t], pE[t] + 1024);
        SB0();
        g1_steps<0>(wf, acc, xa, xb, pE, pO, st4, gc);
    }
    load_wf<14, 22>(wf, gc.voffW, gc.wD, gc.wD);
    STAMP(16);
    *(LDS float*)(lds + O_UBS + tid * 4) = __uint_as_float(ub0);
    const float ssum = half_add(st4[0] + st4[1]), ssq = half_add(st4[2] + st4[3]);
    if (lane < 32) *(LDS f32x2*)(lds + O_ST + (par * 128 + wq * 32 + lane) * 8) = f32x2{ssum, ssq};
    bar();
    {
        const int h = lane >> 5;
        f32x4 ug[2], bg[2], uu[2], bu[2];
#pragma unroll
        for (int k = 0; k < 2; ++k) {
            LDS char* pu = lds + O_UBS + (wid * 32 + 8 * k + 4 * h) * 4;
            ug[k] = *(LDS f32x4*)(pu); uu[k] = *(LDS f32x4*)(pu + 64);
            bg[k] = *(LDS f32x4*)(pu + 1024); bu[k] = *(LDS f32x4*)(pu + 1024 + 64);
        }
#pragma unroll
        for (int t = 0; t < 4; ++t) {
            const int ta = t ^ wq;
            const f32x2 v0 = *(LDS f32x2*)(lds + O_ST + (32 * ta + (lane & 31)) * 8), v1 = *(LDS f32x2*)(lds + O_ST + (128 + 32 * ta + (lane & 31)) * 8);
            const float mean = (v0.x + v1.x) * (1.f / D_);
            const float var = fmaxf((v0.y + v1.y) * (1.f / D_) - mean * mean, 0.f);
            const float r_ = __builtin_amdgcn_rcpf(rbf(__builtin_amdgcn_sqrtf(rbf(rbf(var) + EPS)))), nm = -rbf(mean) * r_;
            float hv[8];
#pragma unroll
            for (int r = 0; r < 8; ++r) {
                const float gg = r_ * acc[t][r] + (nm * ug[r >> 2][r & 3] + bg[r >> 2][r & 3]);
                const float up = r_ * acc[t][r + 8] + (nm * uu[r >> 2][r & 3] + bu[r >> 2][r & 3]);
                hv[r] = gg * up;
            }
            const u32x4 o = {pk2(hv[0], hv[1]), pk2(hv[2], hv[3]), pk2(hv[4], hv[5]), pk2(hv[6], hv[7])};
            st16_c(lane * 16, ws + WS_HF + (size_t)((4 * mi + ta) * 112 + 8 * ni + wid) * 1024, o);
        }
    }
    STAMP(17);
    wait_vm_bar<0>();
    STAMP(18);
    if (wid == 0 && lane < 14) {
        const int xc2 = 2 * (mi >> 2) + (lane >= 7 ? 1 : 0), q2 = 4 * (lane >= 7 ? lane - 7 : lane) + (mi & 3);
        st8_c((unsigned)((xc2 * 28 + q2) * 128 + ni * 8), ws + WS_FD, tagv);
    }
    {
        const unsigned voffX = (unsigned)((lane >> 3) * L_ + 8 * (lane & 7)) * 2;
        glds16_c(voffX, ws + WS_X1T + ((size_t)(64 * ni2 + 8 * wid) * L_ + 64 * mi2) * 2, ldsb + O_X2 + 8 * wid * 128);
        load_wf<22, 28>(wf, gc.voffW, gc.wD, gc.wD);
    }
    if (wid == 0) poll_flags<1>(ws + WS_FD + (size_t)j * 128, (lane & 7) * 16, lane < 7, tag);
    bar();
    STAMP(19);
    f32x16 acc2[2] = {};
    {
        const char* baseH = ws + WS_HF + (size_t)((2 * mi2 + dh) * 112 + 28 * kq) * 1024;
        const unsigned ldsH = ldsb + (kq * 2 + dh) * 2048;
        const unsigned voff = lane * 16;
        g2_prologue<0>(voff, baseH, ldsH);
        wait_vm_bar<2 * (G2_AHEAD - 1)>();
        pin_range<0, 27>(wf);
        STAMP(20);
        u32x4 ha[4], hb[4];
        LDS char* prd = lds + kq * 4096 + lane * 16;
        g2_read<0>(ha, prd);
        g2_chunks<0>(wf, acc2, ha, hb, prd, voff, baseH, ldsH);
    }
    STAMP(21);
    bar();
    {
#pragma unroll
        for (int t = 0; t < 4; ++t) {
            if (t != kq) {
                LDS char* dst = lds + ((dh * 4 + t) * 3 + (kq > t ? kq - 1 : kq)) * 2048 + lane * 16;
                const f32x16 v = acc2[t >> 1];
                const int o = 8 * (t & 1);
                *(LDS f32x4*)(dst) = f32x4{v[o], v[o + 1], v[o + 2], v[o + 3]};
                *(LDS f32x4*)(dst + 1024) = f32x4{v[o + 4], v[o + 5], v[o + 6], v[o + 7]};
            }
        }
        bar();
        const f32x16 own = (kq >> 1) ? acc2[1] : acc2[0];
        f32x4 y0, y1;
        if (kq & 1) { y0 = f32x4{own[8], own[9], own[10], own[11]}; y1 = f32x4{own[12], own[13], own[14], own[15]}; }
        else { y0 = f32x4{own[0], own[1], own[2], own[3]}; y1 = f32x4{own[4], own[5], own[6], own[7]}; }
#pragma unroll
        for (int s = 0; s < 3; ++s) {
            LDS char* src = lds + ((dh * 4 + kq) * 3 + s) * 2048 + lane * 16;
            y0 += *(LDS f32x4*)(src); y1 += *(LDS f32x4*)(src + 1024);
        }
        const int h = lane >> 5, blk = (lane >> 4) & 1, qq = (lane & 15) >> 2, p = lane & 3;
        const int lt = kq >> 1, g0 = 2 * (kq & 1);
        const int lrow = 64 * mi2 + 32 * lt + (lane & 31);
#pragma unroll
        for (int k = 0; k < 2; ++k) {
            const int gg = g0 + k;
            LDS char* px = lds + O_X2 + (32 * dh + 8 * gg + 4 * h + qq) * 128 + (32 * lt + 16 * blk + 4 * p) * 2;
            const s16x4 xr = __builtin_amdgcn_ds_read_tr16_b64_v4i16((LDS s16x4*)px);
            const f32x4 gb = *(LDS f32x4*)(lds + O_GB + (32 * dh + 8 * gg + 4 * h) * 4);
            const f32x4 y = k ? y1 : y0;
            float o[4];
#pragma unroll
            for (int e = 0; e < 4; ++e) o[e] = bf2f((u16)xr[e]) + gb[e] * y[e];
            *(u32x2*)(out + (size_t)lrow * D_ + 64 * ni2 + 32 * dh + 8 * gg + 4 * h) = u32x2{pk2(o[0], o[1]), pk2(o[2], o[3])};
        }
    }
    if (b == 0 && tid == 0) st8_c(0u, ws + WS_EP, tagv);
    STAMP(22);
    DIAG_END;
}

extern "C" void kernel_launch(void* const* d_in, const int* in_sizes, int n_in,
                              void* d_out, int out_size, void* d_ws, size_t ws_size,
                              hipStream_t stream) {
    (void)hipFuncSetAttribute((const void*)fused_block, hipFuncAttributeMaxDynamicSharedMemorySize, LDS_BYTES);
    fused_block<<<256, 512, LDS_BYTES, stream>>>(
        (const u16*)d_in[0], (const u16*)d_in[1], (const u16*)d_in[2], (const u16*)d_in[3], (const u16*)d_in[4],
        (const u16*)d_in[5], (const u16*)d_in[6], (const u16*)d_in[7], (const u16*)d_in[8],
        (const u16*)d_in[10], (const u16*)d_in[11], (const u16*)d_in[12], (const u16*)d_in[13], (const u16*)d_in[14],
        (char*)d_ws, (u16*)d_out);
}
```

```cpp
#include <hip/hip_runtime.h>
#include <hip/hip_bf16.h>
#include <stdint.h>

typedef unsigned short u16;
typedef unsigned long long u64;
typedef __attribute__((ext_vector_type(8))) __bf16 bf16x8;
typedef __attribute__((ext_vector_type(2))) __bf16 bf16x2;
typedef __attribute__((ext_vector_type(4))) short s16x4;
typedef __attribute__((ext_vector_type(4))) float f32x4;
typedef __attribute__((ext_vector_type(2))) float f32x2;
typedef __attribute__((ext_vector_type(16))) float f32x16;
typedef __attribute__((ext_vector_type(4))) unsigned u32x4;
typedef __attribute__((ext_vector_type(2))) unsigned u32x2;
#define LDS __attribute__((address_space(3)))
#define DI __device__ __forceinline__

constexpr int L_ = 2048, D_ = 448, NM = 256, F_ = 1792;
constexpr float EPS = 1e-5f;
#define POLL_A_DELAY 8
#define POLL_GAP 6
#define POLL_C_DELAY 26
#define POLL_D_DELAY 0

constexpr size_t WS_EP = 0;
constexpr size_t WS_FA = 4096;
constexpr size_t WS_FB = WS_FA + 8 * 2048;
constexpr size_t WS_FC = WS_FB + 8 * 2048;
constexpr size_t WS_FD = WS_FC + 8 * 2048;
constexpr size_t WS_STATS = WS_FD + 224 * 128;
constexpr size_t WS_MOD = WS_STATS + L_ * 4;
constexpr size_t WS_UB = WS_MOD + 4 * 2688 * 4;
constexpr size_t WS_XT = WS_UB + 2 * 3584 * 4;
constexpr size_t WS_X1T = WS_XT + (size_t)D_ * L_ * 2;
constexpr size_t WS_WPF = WS_X1T + (size_t)D_ * L_ * 2;
constexpr size_t WS_WDF = WS_WPF + (size_t)112 * 28 * 1024;
constexpr size_t WS_HF = WS_WDF + (size_t)14 * 112 * 1024;
constexpr size_t WS_END = WS_HF + (size_t)64 * 112 * 1024;
static_assert(WS_STATS % 256 == 0 && WS_MOD % 256 == 0 && WS_UB % 128 == 0 && WS_X1T % 128 == 0, "alignment");

constexpr int T_ROW = 1056;
constexpr int T_CH = 57 * T_ROW;
constexpr int O_KC = 2 * T_CH;
constexpr int KC_CH = 772 * 4;
constexpr int O_MR = O_KC + 2 * KC_CH;
constexpr int O_SB = O_MR + 2048;
constexpr int O_OK = O_SB + 64;
constexpr int O_WS = O_OK + 64;
constexpr int O_MD = O_WS + 16 * 896;
constexpr int O_GB = O_MD + 64;
constexpr int LDS_BYTES = 147456;
static_assert(O_GB + 256 <= LDS_BYTES && O_GB >= 131072 + 8192, "lds map");
constexpr int RC = 4160, RCH = 8 * RC;
constexpr int O_H = 2 * RCH;
constexpr int HCH = (256 + 2048) * 2;
constexpr int O_XR = O_H + 2 * HCH;
constexpr int O_RED = O_XR + 2 * 4096;
static_assert(O_RED + 16384 <= O_KC, "conv buffers must not reach the K table");
constexpr int O_ST = 448 * 256;
constexpr int O_RS = O_ST + 2048;
constexpr int O_UBS = O_RS + 1024;
constexpr int O_X2 = 131072;
static_assert(O_UBS + 2048 <= O_X2 && O_X2 + 8192 <= LDS_BYTES, "lds map");

DI float bf2f(unsigned h) { return __uint_as_float(h << 16); }
DI float bflo(unsigned u) { return __uint_as_float(u << 16); }
DI float bfhi(unsigned u) { return __uint_as_float(u & 0xffff0000u); }
DI unsigned pk2(float a, float b) { bf16x2 r = {(__bf16)a, (__bf16)b}; return __builtin_bit_cast(unsigned, r); }
DI float rbf(float a) { return (float)(__bf16)a; }
DI bf16x2 asb2(unsigned u) { return __builtin_bit_cast(bf16x2, u); }
DI float dot2(unsigned a, unsigned b, float c) { return __builtin_amdgcn_fdot2_f32_bf16(asb2(a), asb2(b), c, false); }

template <int CTRL, int RM>
DI float dpp_add(float v) {
    int t = __builtin_amdgcn_update_dpp(0, __float_as_int(v), CTRL, RM, 0xf, true);
    return v + __int_as_float(t);
}
DI float wave_sum(float v) {
    v = dpp_add<0x111, 0xf>(v);
    v = dpp_add<0x112, 0xf>(v);
    v = dpp_add<0x114, 0xf>(v);
    v = dpp_add<0x118, 0xf>(v);
    v = dpp_add<0x142, 0xa>(v);
    v = dpp_add<0x143, 0xc>(v);
    return __int_as_float(__builtin_amdgcn_readlane(__float_as_int(v), 63));
}
DI float swap32_add(float a, float b) {
    auto r = __builtin_amdgcn_permlane32_swap(__float_as_uint(a), __float_as_uint(b), false, false);
    return __uint_as_float(r[0]) + __uint_as_float(r[1]);
}
DI float swap16_add(float a, float b) {
    auto r = __builtin_amdgcn_permlane16_swap(__float_as_uint(a), __float_as_uint(b), false, false);
    return __uint_as_float(r[0]) + __uint_as_float(r[1]);
}
DI float half_add(float a) {
    auto r = __builtin_amdgcn_permlane32_swap(__float_as_uint(a), __float_as_uint(a), false, false);
    return __uint_as_float(r[0]) + __uint_as_float(r[1]);
}
DI f32x4 mfma16(bf16x8 a, bf16x8 b, f32x4 c) { return __builtin_amdgcn_mfma_f32_16x16x32_bf16(a, b, c, 0, 0, 0); }
DI f32x16 mfma32(bf16x8 a, bf16x8 b, f32x16 c) { return __builtin_amdgcn_mfma_f32_32x32x16_bf16(a, b, c, 0, 0, 0); }
DI bf16x8 as_frag(u32x4 v) { return __builtin_bit_cast(bf16x8, v); }
DI bf16x8 tr_frag(LDS char* p0, LDS char* p1) {
    s16x4 a = __builtin_amdgcn_ds_read_tr16_b64_v4i16((LDS s16x4*)p0);
    s16x4 b = __builtin_amdgcn_ds_read_tr16_b64_v4i16((LDS s16x4*)p1);
    return __builtin_bit_cast(bf16x8, __builtin_shufflevector(a, b, 0, 1, 2, 3, 4, 5, 6, 7));
}
#define SB0() __builtin_amdgcn_sched_barrier(0)

#define LD_ASM(name, inst, T)                                                                                   \
    template <int OFF = 0> DI void name(T& dst, unsigned voff, const void* sbase) {                             \
        asm volatile(inst " %0, %1, %2 offset:%3" : "=v"(dst) : "v"(voff), "s"(sbase), "i"(OFF));               \
    }                                                                                                           \
    template <int OFF = 0> DI void name##_c(T& dst, unsigned voff, const void* sbase) {                         \
        asm volatile(inst " %0, %1, %2 offset:%3 sc1" : "=v"(dst) : "v"(voff), "s"(sbase), "i"(OFF));           \
    }
LD_ASM(ld16, "global_load_dwordx4", u32x4)
LD_ASM(ld8, "global_load_dwordx2", u32x2)
LD_ASM(ld4, "global_load_dword", unsigned)
LD_ASM(ld2, "global_load_ushort", unsigned)
DI void ld4_c_io(unsigned& dst, unsigned voff, const void* sbase) {
    asm volatile("global_load_dword %0, %1, %2 sc1" : "+v"(dst) : "v"(voff), "s"(sbase));
}
DI void ld16v(u32x4& dst, const void* p) { asm volatile("global_load_dwordx4 %0, %1, off" : "=v"(dst) : "v"(p)); }
DI void st16_c(unsigned voff, const void* sbase, u32x4 v) {
    asm volatile("global_store_dwordx4 %0, %1, %2 sc1\n\ts_nop 1" :: "v"(voff), "v"(v), "s"(sbase) : "memory");
}
DI void st8_c(unsigned voff, const void* sbase, u32x2 v) {
    asm volatile("global_store_dwordx2 %0, %1, %2 sc1\n\ts_nop 1" :: "v"(voff), "v"(v), "s"(sbase) : "memory");
}
DI void st4_c(unsigned voff, const void* sbase, unsigned v) {
    asm volatile("global_store_dword %0, %1, %2 sc1\n\ts_nop 1" :: "v"(voff), "v"(v), "s"(sbase) : "memory");
}
DI void glds16_c(unsigned voff, const void* sbase, unsigned ldsdst) {
    unsigned keep;
    asm volatile("s_mov_b32 %0, m0\n\ts_mov_b32 m0, %3\n\ts_nop 0\n\tglobal_load_lds_dwordx4 %1, %2 sc1\n\ts_mov_b32 m0, %0"
                 : "=&s"(keep) : "v"(voff), "s"(sbase), "s"(ldsdst) : "memory");
}
DI void glds16(unsigned voff, const void* sbase, unsigned ldsdst) {
    unsigned keep;
    asm volatile("s_mov_b32 %0, m0\n\ts_mov_b32 m0, %3\n\ts_nop 0\n\tglobal_load_lds_dwordx4 %1, %2\n\ts_mov_b32 m0, %0"
                 : "=&s"(keep) : "v"(voff), "s"(sbase), "s"(ldsdst) : "memory");
}
template <int N> DI void wait_vm() { asm volatile("s_waitcnt vmcnt(%0)" :: "i"(N) : "memory"); }
DI void wait_vm0_cond() { asm volatile("s_waitcnt vmcnt(0) ; COND" ::: "memory"); }
template <int N> DI void wait_vm_bar() { asm volatile("s_waitcnt vmcnt(%0) lgkmcnt(0)\n\ts_barrier" :: "i"(N) : "memory"); }
DI void bar() { asm volatile("s_waitcnt lgkmcnt(0)\n\ts_barrier" ::: "memory"); }
template <class T> DI void pin(T& a) { asm volatile("" : "+v"(a)); }

template <int NP>
DI void poll_flags(const char* base, unsigned voff, bool lane_on, u64 tag) {
    const unsigned tl = (unsigned)tag, th = (unsigned)(tag >> 32);
    for (int spin = 0; spin < (1 << 15); ++spin) {
        u32x4 f0, f1;
        if (NP == 2)
            asm volatile("global_load_dwordx4 %0, %2, %3 sc1\n\tglobal_load_dwordx4 %1, %2, %3 offset:1024 sc1\n\ts_waitcnt vmcnt(0) ; COND"
                         : "=&v"(f0), "=&v"(f1) : "v"(voff), "s"(base) : "memory");
        else {
            asm volatile("global_load_dwordx4 %0, %1, %2 sc1\n\ts_waitcnt vmcnt(0) ; COND" : "=&v"(f0) : "v"(voff), "s"(base) : "memory");
            f1 = f0;
        }
        const bool ok = (f0.x == tl && f0.y == th && f0.z == tl && f0.w == th && f1.x == tl && f1.y == th && f1.z == tl && f1.w == th) || !lane_on;
        if (__all(ok)) break;
        __builtin_amdgcn_s_sleep(POLL_GAP);
    }
}

#define DIAG_PARAM
#define DIAG_FWD
#define G1STAMP(S)
#define DIAG_DECL
#define STAMP(i)
#define DIAG_END

typedef f32x2 cf;
DI cf cmul(cf a, cf b) {
    cf r;
    asm("v_pk_mul_f32 %0, %1, %2 op_sel:[0,0] op_sel_hi:[0,1]\n\t"
        "v_pk_fma_f32 %0, %1, %2, %0 op_sel:[1,1,0] op_sel_hi:[1,0,1] neg_lo:[1,0,0]"
        : "=&v"(r) : "v"(a), "v"(b));
    return r;
}
DI cf csq(cf a) { return cmul(a, a); }
DI cf zpow(float a2, float br, float l) {
    const float mag = __builtin_amdgcn_exp2f(a2 * l);
    const float fr = __builtin_amdgcn_fractf(br * l);
    return cf{mag * __builtin_amdgcn_cosf(fr), mag * __builtin_amdgcn_sinf(fr)};
}

struct KIdx { u16 v[2080]; };
constexpr KIdx make_kidx() {
    KIdx t{};
    for (int idx = 0; idx < 2080; ++idx) {
        if (idx >= 2048) { t.v[idx] = 768 * 4; continue; }
        const int m = 2047 - idx;
        int sh = 0;
        while ((m >> sh) >= 256) ++sh;
        int l = m;
        if (sh > 0) {
            int q = m >> sh; const int rem = m & ((1 << sh) - 1), half = 1 << (sh - 1);
            if (rem > half || (rem == half && (q & 1))) ++q;
            l = q << sh;
        }
        const int hi7 = l >> 7;
        int row = 0;
        if (l < 256) row = 8 * hi7 + (l & 7);
        else if (l < 512) row = 8 + 4 * hi7 + ((l >> 1) & 3);
        else if (l < 1024) row = 16 + 2 * hi7 + ((l >> 2) & 1);
        else row = 24 + hi7;
        t.v[idx] = (u16)((row * 16 + ((l >> 3) & 15)) * 4);
    }
    return t;
}
__device__ const KIdx g_kidx = make_kidx();

template <int TG, int OFF>
DI void conv_load(LDS char* pa, LDS char* pb, bf16x8 (&A)[8], bf16x8 (&B)[8]) {
    constexpr int NU = TG == 0 ? 8 : 7;
#pragma unroll
    for (int u = 0; u < NU; ++u) {
        A[u] = as_frag(*(LDS u32x4*)(pa + 512 * (7 - u) + OFF));
        B[u] = as_frag(*(LDS u32x4*)(pb + 512 * u + OFF));
    }
}
template <int TG>
DI void conv_mma(const bf16x8 (&A)[8], const bf16x8 (&B)[8], f32x4 (&acc)[4]) {
    constexpr int T0 = TG == 0 ? 0 : 1, T1 = TG == 0 ? 3 : 2, T2 = TG == 0 ? 4 : 5, T3 = TG == 0 ? 7 : 6;
#pragma unroll
    for (int u = 0; u <= T3; ++u) {
        acc[3] = mfma16(A[u], B[T3 - u], acc[3]);
        if (u <= T2) acc[2] = mfma16(A[u], B[T2 - u], acc[2]);
        if (u <= T1) acc[1] = mfma16(A[u], B[T1 - u], acc[1]);
        if (u <= T0) acc[0] = mfma16(A[u], B[T0 - u], acc[0]);
    }
}
DI bool flags_ok(const u32x4& f, u64 tag, bool lane_on) {
    const unsigned tl = (unsigned)tag, th = (unsigned)(tag >> 32);
    return (f.x == tl && f.y == th && f.z == tl && f.w == th) || !lane_on;
}
template <int TG>
DI void conv_run(LDS char* pa, LDS char* pb, f32x4 (&acc)[4]) {
    bf16x8 A0[8], B0[8], A1[8], B1[8];
    conv_load<TG, 192>(pa, pb, A0, B0);
    conv_load<TG, 128>(pa, pb, A1, B1); SB0();
    conv_mma<TG>(A0, B0, acc); SB0();
    conv_load<TG, 64>(pa, pb, A0, B0); SB0();
    conv_mma<TG>(A1, B1, acc); SB0();
    conv_load<TG, 0>(pa, pb, A1, B1); SB0();
    conv_mma<TG>(A0, B0, acc); SB0();
    conv_mma<TG>(A1, B1, acc);
}

struct G1Sched { int op[96]; int at[96]; int n; };
constexpr G1Sched make_g1() {
    G1Sched s{}; int n = 0;
    const int b0[2] = {100, 0}, b1[6] = {101, 1, 2, 102, 3, 103};
    for (int i = 0; i < 2; ++i) { s.op[n] = b0[i]; s.at[n] = -2; ++n; }
    for (int i = 0; i < 6; ++i) { s.op[n] = b1[i]; s.at[n] = -1; ++n; }
    for (int t = 0; t < 10; ++t) { s.op[n] = 4 + t; s.at[n] = t; ++n; s.op[n] = 104 + t; s.at[n] = t; ++n; }
    for (int t = 10; t < 17; ++t) { s.op[n] = 114 + 2 * (t - 10); s.at[n] = t; ++n; s.op[n] = 115 + 2 * (t - 10); s.at[n] = t; ++n; }
    for (int t = 17; t < 28; ++t) { s.op[n] = 200 + (t - 17); s.at[n] = t; ++n; }
    s.n = n;
    return s;
}
constexpr G1Sched G1S = make_g1();
constexpr int G1_D_INLOOP = 11;
constexpr int g1_need_x(int st) { return st == -1 ? 0 : st == 1 ? 3 : st == 7 ? 7 : st == 15 ? 13 : -1; }
constexpr int g1_wait(int st) {
    const int lim = st < 0 ? 0 : st, needw = st < 0 ? 0 : st;
    int issued = 0, last = -1;
    for (int i = 0; i < G1S.n; ++i) {
        if (G1S.at[i] >= lim) break;
        const int o = G1S.op[i];
        if ((o < 100 && o <= g1_need_x(st)) || (o >= 100 && o < 200 && o - 100 <= needw)) last = issued;
        ++issued;
    }
    return issued - 1 - last;
}
static_assert(g1_wait(-1) == 6 && g1_wait(1) == 3 && g1_wait(4) == 6, "gemm1 schedule");
struct G1Ctx { unsigned voffA; const char* baseA; unsigned ldsA; unsigned voffW; const char* wW; const char* wD; };
template <int I, int AT>
DI void g1_issue(u32x4 (&wf)[28], const G1Ctx& c) {
    if constexpr (I < G1S.n) {
        if constexpr (G1S.at[I] == AT) {
            constexpr int o = G1S.op[I];
            if constexpr (o < 100) glds16_c(c.voffA, c.baseA + (size_t)(32 * o) * (L_ * 2), c.ldsA + 32 * o * 256);
            else if constexpr (o < 200) ld16_c<((o - 100) & 3) * 1024>(wf[o - 100], c.voffW, c.wW + ((o - 100) >> 2) * 4096);
            else ld16_c<((o - 200) & 3) * 1024>(wf[o - 200], c.voffW, c.wD + ((o - 200) >> 2) * 4096);
        }
        g1_issue<I + 1, AT>(wf, c);
    }
}
template <int K0, int K1>
DI void pin_range(u32x4 (&wf)[28]) { if constexpr (K0 <= K1) { pin(wf[K0]); pin_range<K0 + 1, K1>(wf); } }
template <int S>
DI void g1_steps(u32x4 (&wf)[28], f32x16 (&acc)[4], bf16x8 (&cur)[4], bf16x8 (&nxt)[4], LDS char* (&p0)[4], LDS char* (&p1)[4],
                 float (&st)[4], const G1Ctx& c DIAG_PARAM) {
    if constexpr (S < 28) {
        if constexpr (S > 0) {
            if constexpr (g1_need_x(S) >= 0) { wait_vm_bar<g1_wait(S)>(); G1STAMP(S); } else wait_vm<g1_wait(S)>();
            pin(wf[S]);
        }
        if constexpr (S == 15) {
#pragma unroll
            for (int t = 0; t < 4; ++t) {
                asm volatile("v_add_u32 %0, 0x10000, %0" : "+v"(p0[t]));
                asm volatile("v_add_u32 %0, 0x10000, %0" : "+v"(p1[t]));
            }
        }
        const u32x4 w = wf[S];
        const u32x4 xu = __builtin_bit_cast(u32x4, cur[0]);
        const unsigned d0 = xu.x, d1 = xu.y;
        float lo = 0.f, hi = 0.f;
#pragma unroll
        for (int t = 0; t < 4; ++t) {
            acc[t] = mfma32(as_frag(w), cur[t], acc[t]);
            if constexpr (S + 1 < 28) {
                constexpr int off = (S + 1 >= 16 ? S + 1 - 16 : S + 1) * 4096;
                s16x4 a = __builtin_amdgcn_ds_read_tr16_b64_v4i16((LDS s16x4*)(p0[t] + off));
                s16x4 b = __builtin_amdgcn_ds_read_tr16_b64_v4i16((LDS s16x4*)(p1[t] + off));
                nxt[t] = __builtin_bit_cast(bf16x8, __builtin_shufflevector(a, b, 0, 1, 2, 3, 4, 5, 6, 7));
            }
            if (t == 0) { lo = bflo(d0); hi = bfhi(d0); st[0] += lo; }
            if (t == 1) { st[1] += hi; st[2] = fmaf(lo, lo, st[2]); st[3] = fmaf(hi, hi, st[3]); }
            if (t == 2) { lo = bflo(d1); hi = bfhi(d1); st[0] += lo; }
            if (t == 3) { st[1] += hi; st[2] = fmaf(lo, lo, st[2]); st[3] = fmaf(hi, hi, st[3]); }
            SB0();
            if (t == 0) { g1_issue<0, S>(wf, c); SB0(); }
        }
        g1_steps<S + 1>(wf, acc, nxt, cur, p0, p1, st, c DIAG_FWD);
    }
}

constexpr int G2_AHEAD = 5, G2_RING = 8;
template <int C, int I>
DI void g2_issue1(unsigned voff, const char* baseH, unsigned ldsH) {
    glds16_c(voff, baseH + (size_t)(2 * C + I) * 1024, ldsH + (C % G2_RING) * 16384 + I * 1024);
}
template <int C>
DI void g2_prologue(unsigned voff, const char* baseH, unsigned ldsH) {
    if constexpr (C < G2_AHEAD) { g2_issue1<C, 0>(voff, baseH, ldsH); g2_issue1<C, 1>(voff, baseH, ldsH); g2_prologue<C + 1>(voff, baseH, ldsH); }
}
template <int C>
DI void g2_chunks(u32x4 (&wf)[28], f32x16 (&acc)[2], u32x4 (&ha)[4], u32x4 (&hb)[4], LDS char* prd,
                  unsigned voff, const char* baseH, unsigned ldsH) {
    if constexpr (C < 14) {
        acc[0] = mfma32(as_frag(wf[2 * C]), as_frag(ha[0]), acc[0]); SB0();
        if constexpr (C + 1 < 14) {
            constexpr int issued = (C + G2_AHEAD < 14) ? C + G2_AHEAD : 14;
            wait_vm_bar<2 * (issued - (C + 2))>();
        }
        acc[1] = mfma32(as_frag(wf[2 * C]), as_frag(ha[2]), acc[1]);
        LDS char* q = prd + ((C + 1) % G2_RING) * 16384;
        if constexpr (C + 1 < 14) { hb[0] = *(LDS u32x4*)(q); hb[2] = *(LDS u32x4*)(q + 2048); }
        SB0();
        if constexpr (C + G2_AHEAD < 14) { g2_issue1<C + G2_AHEAD, 0>(voff, baseH, ldsH); SB0(); }
        acc[0] = mfma32(as_frag(wf[2 * C + 1]), as_frag(ha[1]), acc[0]);
        if constexpr (C + 1 < 14) { hb[1] = *(LDS u32x4*)(q + 1024); hb[3] = *(LDS u32x4*)(q + 3072); }
        SB0();
        if constexpr (C + G2_AHEAD < 14) { g2_issue1<C + G2_AHEAD, 1>(voff, baseH, ldsH); SB0(); }
        acc[1] = mfma32(as_frag(wf[2 * C + 1]), as_frag(ha[3]), acc[1]);
        SB0();
        g2_chunks<C + 1>(wf, acc, hb, ha, prd, voff, baseH, ldsH);
    }
}
template <int S, int E>
DI void load_wf(u32x4 (&wf)[28], unsigned voff, const char* base) {
    if constexpr (S < E) {
        ld16_c<(S & 3) * 1024>(wf[S], voff, base + (S >> 2) * 4096);
        load_wf<S + 1, E>(wf, voff, base);
    }
}

__global__ __launch_bounds__(512, 2) void fused_block(
    const u16* __restrict__ x, const u16* __restrict__ Are, const u16* __restrict__ Aim,
    const u16* __restrict__ Bre, const u16* __restrict__ Bim, const u16* __restrict__ Cre,
    const u16* __restrict__ Cim, const u16* __restrict__ logdt, const u16* __restrict__ dskip,
    const u16* __restrict__ wg, const u16* __restrict__ wu, const u16* __restrict__ wd,
    const u16* __restrict__ cvec, const u16* __restrict__ wmod, char* __restrict__ ws, u16* __restrict__ out)
{
    DIAG_DECL STAMP(0);
    extern __shared__ __attribute__((aligned(16))) char smem[];
    LDS char* lds = (LDS char*)smem;
    const unsigned ldsb = (unsigned)(size_t)lds;
    const int tid = threadIdx.x, lane = tid & 63;
    const int wid = __builtin_amdgcn_readfirstlane(tid >> 6);
    const int b = blockIdx.x, xc = b & 7, q = b >> 3;
    const bool mainb = q < 28;
    const int j = xc * 28 + q;
    const int me = xc * 32 + q;
    const bool doB = q < 21;
    const int gB = xc * 21 + (doB ? q : q - 21), cgB = gB >> 2, kqB = gB & 3;

    u32x2 epv;
    ld8_c(epv, 0u, ws + WS_EP);
    u32x4 xv;
    ld16(xv, (unsigned)(lane < 56 ? lane : 55) * 16, x + (size_t)(8 * me + wid) * D_);
    const int ch = wid >> 2, n = tid & 255, dch = mainb ? 2 * j + ch : ch;
    unsigned pAre, pAim, pBre, pBim, pCre, pCim, pdt;
    {
        const unsigned o = (unsigned)(dch * NM + n) * 2;
        ld2(pAre, o, Are); ld2(pAim, o, Aim); ld2(pBre, o, Bre); ld2(pBim, o, Bim); ld2(pCre, o, Cre); ld2(pCim, o, Cim);
        ld2(pdt, 0u, logdt + dch);
    }
    u32x4 w0, w1; unsigned c0, c1;
    const int kB0 = tid >> 3, kB1 = kB0 + 64 < 112 ? kB0 + 64 : kB0;
    {
        const u16* wb = wmod + (size_t)(112 * kqB) * 2688 + 64 * cgB;
        ld2(c0, kB0 * 2, cvec + 112 * kqB); ld2(c1, kB1 * 2, cvec + 112 * kqB);
        ld16(w0, (unsigned)(kB0 * 2688 + 8 * (tid & 7)) * 2, wb); ld16(w1, (unsigned)(kB1 * 2688 + 8 * (tid & 7)) * 2, wb);
    }
    unsigned mdk;
    ld4(mdk, 0u, dskip + (mainb ? 2 * j : 0));
    u32x4 kx0, kx1;
    ld16(kx0, n * 16, g_kidx.v); ld16<16>(kx1, n * 16, g_kidx.v);
    STAMP(1);
    wait_vm<14>();
    STAMP(25);
    pin(xv); pin(epv);
    const u64 tag = (((u64)epv.y << 32) | epv.x) + 1;
    const u32x2 tagv = {(unsigned)tag, (unsigned)(tag >> 32)};
    {
        if (lane < 56) *(LDS u32x4*)(lds + O_WS + wid * 896 + lane * 16) = xv;
        else xv = u32x4{0, 0, 0, 0};
        const unsigned x0 = xv.x, x1 = xv.y, x2 = xv.z, x3 = xv.w;
        const float sm = dot2(x0, 0x3f803f80u, dot2(x1, 0x3f803f80u, 0.f)) + dot2(x2, 0x3f803f80u, dot2(x3, 0x3f803f80u, 0.f));
        const float sq = dot2(x0, x0, dot2(x1, x1, 0.f)) + dot2(x2, x2, dot2(x3, x3, 0.f));
        float t = swap32_add(sm, sq);
        t = dpp_add<0x111, 0xf>(t); t = dpp_add<0x112, 0xf>(t); t = dpp_add<0x114, 0xf>(t); t = dpp_add<0x118, 0xf>(t);
        t = dpp_add<0x142, 0xa>(t);
        const float mean = __int_as_float(__builtin_amdgcn_readlane(__float_as_int(t), 31)) * (1.f / D_);
        const float var = fmaxf(__int_as_float(__builtin_amdgcn_readlane(__float_as_int(t), 63)) * (1.f / D_) - mean * mean, 0.f);
        if (lane == 0) *(LDS unsigned*)(lds + O_SB + wid * 4) = pk2(mean, __builtin_amdgcn_sqrtf(rbf(rbf(var) + EPS)));
    }
    STAMP(26);
    wait_vm<3>();
    STAMP(27);
    pin(c0); pin(c1); pin(w0); pin(w1);
    pin(pAre); pin(pAim); pin(pBre); pin(pBim); pin(pCre); pin(pCim); pin(pdt);
    if (doB) {
        const float f0 = bf2f(c0), f1 = bf2f(c1);
        const float s0 = rbf(f0 * __builtin_amdgcn_rcpf(1.f + __expf(-f0)));
        const float s1 = kB0 + 64 < 112 ? rbf(f1 * __builtin_amdgcn_rcpf(1.f + __expf(-f1))) : 0.f;
        float p[8];
        p[0] = s0 * bflo(w0.x) + s1 * bflo(w1.x); p[1] = s0 * bfhi(w0.x) + s1 * bfhi(w1.x);
        p[2] = s0 * bflo(w0.y) + s1 * bflo(w1.y); p[3] = s0 * bfhi(w0.y) + s1 * bfhi(w1.y);
        p[4] = s0 * bflo(w0.z) + s1 * bflo(w1.z); p[5] = s0 * bfhi(w0.z) + s1 * bfhi(w1.z);
        p[6] = s0 * bflo(w0.w) + s1 * bflo(w1.w); p[7] = s0 * bfhi(w0.w) + s1 * bfhi(w1.w);
        const float a0 = swap32_add(p[0], p[4]), a1 = swap32_add(p[1], p[5]), a2 = swap32_add(p[2], p[6]), a3 = swap32_add(p[3], p[7]);
        const float b0 = swap16_add(a0, a2), b1 = swap16_add(a1, a3);
        const bool up8 = (lane & 8) != 0;
        float r = up8 ? b1 : b0;
        const float oth = up8 ? b0 : b1;
        r += __int_as_float(__builtin_amdgcn_update_dpp(0, __float_as_int(oth), 0x128, 0xf, 0xf, true));
        const int col = 8 * (lane & 7) + 4 * (lane >> 5) + 2 * ((lane >> 4) & 1) + ((lane >> 3) & 1);
        *(LDS float*)(lds + O_MR + (wid * 64 + col) * 4) = r;
    }
    bar();
    if (wid == 0) {
        if (lane < 2) st16_c((8 * me + 4 * lane) * 4, ws + WS_STATS, *(LDS u32x4*)(lds + O_SB + lane * 16));
        if (doB) {
            float t = 0.f;
#pragma unroll
            for (int w = 0; w < 8; ++w) t += *(LDS float*)(lds + O_MR + (w * 64 + lane) * 4);
            st4_c((kqB * 2688 + 64 * cgB + lane) * 4, ws + WS_MOD, __float_as_uint(t));
        }
    } else {
        const int d = tid - 64;
        u16 e[8];
#pragma unroll
        for (int i = 0; i < 8; ++i) e[i] = *(LDS u16*)(lds + O_WS + i * 896 + d * 2);
        st16_c((unsigned)d * 16, ws + WS_XT + (size_t)me * 7168,
               u32x4{e[0] | ((unsigned)e[1] << 16), e[2] | ((unsigned)e[3] << 16), e[4] | ((unsigned)e[5] << 16), e[6] | ((unsigned)e[7] << 16)});
    }
    STAMP(2);

    if (!mainb) {
        const int sp = xc * 4 + (q - 28);
        wait_vm_bar<0>();
        if (wid == 0 && lane < 8) st8_c(lane * 2048 + me * 8, ws + WS_FA, tagv);
        if (sp < 28) {
            const int h = lane >> 5, blk = (lane >> 4) & 1, qq = (lane & 15) >> 2, p = lane & 3;
#pragma unroll
            for (int r = 0; r < 7; ++r) glds16(lane * 16, wd + (size_t)(64 * sp) * D_ + (wid + 8 * r) * 512, ldsb + 57344 + (wid + 8 * r) * 1024);
            wait_vm_bar<0>();
#pragma unroll
            for (int r = 0; r < 7; ++r) {
                const int f = wid + 8 * r, s = f / 14, dg = f - 14 * s;
                LDS char* a0 = lds + 57344 + (16 * s + 4 * h + qq) * 896 + (32 * dg + 16 * blk + 4 * p) * 2;
                const u32x4 w = __builtin_bit_cast(u32x4, tr_frag(a0, a0 + 8 * 896));
                st16_c(lane * 16, ws + WS_WDF + (size_t)(dg * 112 + 4 * sp + s) * 1024, w);
            }
            bar();
            STAMP(33);
            {
                const unsigned vo = (unsigned)((lane >> 3) * F_ + 8 * (lane & 7)) * 2;
#pragma unroll
                for (int r = 0; r < 14; ++r) {
                    const int i8 = wid + 8 * r;
                    const int mat = i8 >= 56 ? 1 : 0, row0 = 8 * (i8 - 56 * mat);
                    glds16(vo, (mat ? wu : wg) + (size_t)row0 * F_ + 64 * sp, ldsb + i8 * 1024);
                }
            }
            if (wid == 0) poll_flags<2>(ws + WS_FA + xc * 2048, lane * 16, true, tag);
            bar();
            {
                unsigned mc[4], ms[4];
                const int t2 = tid < 448 ? tid : 447;
#pragma unroll
                for (int k = 0; k < 4; ++k) { ld4_c(mc[k], (unsigned)(k * 2688 + 4 * 448 + t2) * 4, ws + WS_MOD); ld4_c(ms[k], (unsigned)(k * 2688 + 3 * 448 + t2) * 4, ws + WS_MOD); }
                wait_vm<0>();
#pragma unroll
                for (int k = 0; k < 4; ++k) { pin(mc[k]); pin(ms[k]); }
                const float cbv = rbf((__uint_as_float(mc[0]) + __uint_as_float(mc[1])) + (__uint_as_float(mc[2]) + __uint_as_float(mc[3])));
                const float sbv = (__uint_as_float(ms[0]) + __uint_as_float(ms[1])) + (__uint_as_float(ms[2]) + __uint_as_float(ms[3]));
                const unsigned pr = pk2(1.f + cbv, sbv);
                if (tid < 448) { *(LDS u16*)(lds + 114688 + tid * 2) = (u16)pr; *(LDS u16*)(lds + 114688 + 896 + tid * 2) = (u16)(pr >> 16); }
                else *(LDS unsigned*)(lds + 114688 + 1792 + (tid - 448) * 4) = 0u;
            }
            bar();
            {
                const int i = lane & 15, g = lane >> 4, mat = wid >> 2, cg = wid & 3;
                LDS char* pa = lds + 114688 + (i == 0 ? 0 : i == 1 ? 896 : 1792) + g * 16;
                const int astep = i < 2 ? 64 : 0;
                LDS char* pb = lds + mat * 57344 + (8 * g + ((lane & 15) >> 2)) * 128 + cg * 32 + 8 * (lane & 3);
                f32x4 acc = {0.f, 0.f, 0.f, 0.f};
#pragma unroll
                for (int s = 0; s < 14; ++s)
                    acc = mfma16(as_frag(*(LDS u32x4*)(pa + s * astep)), tr_frag(pb + s * 4096, pb + s * 4096 + 512), acc);
                if (lane < 16) {
                    const int f = 64 * sp + 16 * cg + lane;
                    const unsigned o = (unsigned)((f >> 4) * 32 + mat * 16 + (f & 15)) * 4;
                    st4_c(o, ws + WS_UB, __float_as_uint(acc[0]));
                    st4_c(o + 3584 * 4, ws + WS_UB, __float_as_uint(acc[1]));
                }
            }
        }
        wait_vm_bar<0>();
        if (wid == 0 && lane < 8) st8_c(lane * 2048 + me * 8, ws + WS_FC, tagv);
        DIAG_END;
        return;
    }

    u32x4 wr[2];
    {
        const float dt = rbf(__expf(bf2f(pdt)));
        const float a = dt * bf2f(pAre), bb = dt * bf2f(pAim);
        const float dBr = dt * bf2f(pBre), dBi = dt * bf2f(pBim);
        const float fCr = bf2f(pCre), fCi = bf2f(pCim);
        const cf W = cmul(cf{fCr, fCi}, cf{dBr, dBi});
        const float a2 = a * 1.4426950408889634f, br = bb * 0.15915494309189535f;
        LDS char* tb = lds + ch * T_CH + n * 4;
#define PUT(row, v) (*(LDS unsigned*)(tb + (row) * T_ROW) = pk2((v).x, (v).y))
        const cf z1 = zpow(a2, br, 1.f), z8 = zpow(a2, br, 8.f), z128 = zpow(a2, br, 128.f);
        {
            const cf q1 = {z8.x, -z8.y}, q2 = csq(q1), q3 = cmul(q2, q1), q4 = csq(q2), q5 = cmul(q4, q1), q6 = csq(q3), q7 = cmul(q4, q3);
            const cf q8 = csq(q4);
            *(LDS unsigned*)(tb + 41 * T_ROW) = 0x00003f80u;
            PUT(42, q1); PUT(43, q2); PUT(44, q3); PUT(45, q4); PUT(46, q5); PUT(47, q6); PUT(48, q7); PUT(49, q8);
            cf t;
            t = cmul(q8, q1); PUT(50, t); t = csq(q5); PUT(51, t); t = cmul(q8, q3); PUT(52, t);
            t = csq(q6); PUT(53, t); t = cmul(q8, q5); PUT(54, t); t = csq(q7); PUT(55, t); t = cmul(q8, q7); PUT(56, t);
        }
        SB0();
        {
            const cf z2 = csq(z1), z4 = csq(z2);
            const cf z256 = csq(z128), z512 = csq(z256), z1024 = csq(z512);
            cf P[16];
            P[0] = W; P[1] = cmul(W, z1); P[2] = cmul(W, z2); P[3] = cmul(P[2], z1);
            P[4] = cmul(W, z4); P[5] = cmul(P[4], z1); P[6] = cmul(P[4], z2); P[7] = cmul(P[6], z1);
#pragma unroll
            for (int c = 0; c < 8; ++c) P[8 + c] = cmul(P[c], z128);
#pragma unroll
            for (int c = 0; c < 16; ++c) PUT(c, P[c]);
            cf S1[8];
#pragma unroll
            for (int i = 0; i < 8; ++i) { S1[i] = cmul(P[8 * (i >> 2) + 2 * (i & 3)], z256); PUT(16 + i, S1[i]); }
            SB0();
            wait_vm_bar<0>();
            pin(kx0); pin(kx1); pin(mdk);
            if (wid == 0 && lane < 8) st8_c(lane * 2048 + me * 8, ws + WS_FA, tagv);
            {
                const int ks = j >> 3, cgp = j & 7;
    #pragma unroll
                for (int r = 0; r < 2; ++r) {
                    const int c = (tid + 512 * r < 896) ? tid + 512 * r : 895;
                    const int row = c / 56, cc = c - 56 * row, mat = cc >= 28 ? 1 : 0;
                    ld16v(wr[r], (mat ? wu : wg) + (size_t)(16 * ks + row) * F_ + 224 * cgp + 8 * (cc - 28 * mat));
                }
            }
            SB0();
            cf S2[8];
#pragma unroll
            for (int i = 0; i < 8; ++i) {
                const int a_ = i >> 1, c_ = i & 1;
                S2[i] = (a_ < 2) ? cmul(P[8 * a_ + 4 * c_], z512) : cmul(S1[4 * (a_ - 2) + 2 * c_], z512);
                PUT(24 + i, S2[i]);
            }
            cf S30 = {0.f, 0.f};
#pragma unroll
            for (int a_ = 0; a_ < 8; ++a_) {
                const cf src = (a_ < 2) ? P[8 * a_] : (a_ < 4) ? S1[4 * (a_ - 2)] : S2[2 * (a_ - 4)];
                const cf t = cmul(src, z1024);
                if (a_ == 0) S30 = t;
                PUT(32 + a_, t);
            }
            const cf t = cmul(S30, z1024);
            PUT(40, t);
        }
#undef PUT
    }
    if (tid < 2) *(LDS float*)(lds + O_KC + tid * KC_CH + 768 * 4) = 0.f;
    STAMP(3);
    bar();
    STAMP(4);
    const int mi = 4 * (xc >> 1) + (q & 3), ni = 7 * (xc & 1) + (q >> 2);
    const int mi2 = 2 * mi + (xc & 1), ni2 = q >> 2;
    STAMP(23);
    if (wid < 6) {
        const int c2 = wid >= 3 ? 1 : 0, tile = wid - 3 * c2;
        LDS char* tb = lds + c2 * T_CH;
        LDS char* pa = tb + (tile * 16 + (lane & 15)) * T_ROW + (lane >> 4) * 16;
        LDS char* pb = tb + (41 + (lane & 15)) * T_ROW + (lane >> 4) * 16;
        f32x4 acc0 = {0.f, 0.f, 0.f, 0.f}, acc1 = {0.f, 0.f, 0.f, 0.f};
#pragma unroll
        for (int s = 0; s < 16; s += 2) {
            acc0 = mfma16(as_frag(*(LDS u32x4*)(pa + s * 64)), as_frag(*(LDS u32x4*)(pb + s * 64)), acc0);
            acc1 = mfma16(as_frag(*(LDS u32x4*)(pa + s * 64 + 64)), as_frag(*(LDS u32x4*)(pb + s * 64 + 64)), acc1);
        }
        acc0 += acc1;
        LDS float* kc = (LDS float*)(lds + O_KC + c2 * KC_CH) + (tile * 16 + 4 * (lane >> 4)) * 16 + (lane & 15);
#pragma unroll
        for (int e = 0; e < 4; ++e) kc[e * 16] = acc0[e];
    } else if (wid == 7) {
        __builtin_amdgcn_s_sleep(POLL_A_DELAY);
        poll_flags<2>(ws + WS_FA + xc * 2048, lane * 16, true, tag);
    }
    STAMP(24);
    bar();
    STAMP(5);
    u32x4 xq, st0, st1; u32x2 mp; unsigned mq[4] = {0u, 0u, 0u, 0u};
    {
        ld16_c(xq, (unsigned)(tid >> 1) * 7168 + (tid & 1) * 16, ws + WS_XT + (size_t)(2 * j) * 16);
        ld16_c<0>(st0, (tid >> 1) * 32, ws + WS_STATS); ld16_c<16>(st1, (tid >> 1) * 32, ws + WS_STATS);
        const int l12 = lane < 12 ? lane : 0;
        ld8_c(mp, (unsigned)((l12 & 3) * 2688 + (l12 >> 2) * 448 + 2 * j) * 4, ws + WS_MOD);
        const int mo = wid == 7 ? 5 * 448 + 64 * ni2 + lane : 4 * 448 + 16 * (j >> 3) + (lane & 15);
        if (wid == 0 || wid == 7) {
#pragma unroll
            for (int k = 0; k < 4; ++k) ld4_c_io(mq[k], (unsigned)(k * 2688 + mo) * 4, ws + WS_MOD);
        }
    }
    {
        LDS char* kc = lds + O_KC + ch * KC_CH;
        const unsigned ix[8] = {kx0.x, kx0.y, kx0.z, kx0.w, kx1.x, kx1.y, kx1.z, kx1.w};
        unsigned d[8], a[7];
#pragma unroll
        for (int e = 0; e < 8; ++e)
            d[e] = pk2(*(LDS float*)(kc + (ix[e] & 0xffffu)), *(LDS float*)(kc + (ix[e] >> 16)));
#pragma unroll
        for (int e = 0; e < 7; ++e) a[e] = __builtin_amdgcn_alignbit(d[e + 1], d[e], 16);
        LDS char* rc = lds + ch * RCH + 16 * n;
#pragma unroll
        for (int c = 0; c < 4; ++c) {
            *(LDS u32x4*)(rc + (2 * c) * RC) = u32x4{d[c], d[c + 1], d[c + 2], d[c + 3]};
            *(LDS u32x4*)(rc + (2 * c + 1) * RC) = u32x4{a[c], a[c + 1], a[c + 2], a[c + 3]};
        }
        if (n < 8) *(LDS u32x4*)(lds + ch * RCH + n * RC + 16 * 256) = u32x4{0, 0, 0, 0};
        if (n >= 32 && n < 64) *(LDS u32x4*)(lds + O_H + ch * HCH + 16 * (n - 32)) = u32x4{0, 0, 0, 0};
    }
    STAMP(6);
    wait_vm<0>();
    pin(st0); pin(st1); pin(mp); pin(mq[0]); pin(mq[1]); pin(mq[2]); pin(mq[3]); pin(xq); pin(wr[0]); pin(wr[1]);
    *(LDS u32x4*)(lds + O_WS + tid * 16) = wr[0];
    if (tid < 384) *(LDS u32x4*)(lds + O_WS + (tid + 512) * 16) = wr[1];
    STAMP(7);
    float sa0, sa1, ca0_, ca1_, ga0, ga1;
    {
        float m0 = __uint_as_float(mp.x), m1 = __uint_as_float(mp.y);
        m0 = dpp_add<0xb1, 0xf>(m0); m1 = dpp_add<0xb1, 0xf>(m1);
        m0 = dpp_add<0x4e, 0xf>(m0); m1 = dpp_add<0x4e, 0xf>(m1);
        m0 = rbf(m0); m1 = rbf(m1);
        sa0 = __int_as_float(__builtin_amdgcn_readlane(__float_as_int(m0), 0)); sa1 = __int_as_float(__builtin_amdgcn_readlane(__float_as_int(m1), 0));
        ca0_ = __int_as_float(__builtin_amdgcn_readlane(__float_as_int(m0), 4)); ca1_ = __int_as_float(__builtin_amdgcn_readlane(__float_as_int(m1), 4));
        ga0 = __int_as_float(__builtin_amdgcn_readlane(__float_as_int(m0), 8)); ga1 = __int_as_float(__builtin_amdgcn_readlane(__float_as_int(m1), 8));
    }
    {
        const int hc = tid & 1, hn = tid >> 1;
        const unsigned sv[8] = {st0.x, st0.y, st0.z, st0.w, st1.x, st1.y, st1.z, st1.w};
        const float xf[8] = {bflo(xq.x), bfhi(xq.x), bflo(xq.y), bfhi(xq.y), bflo(xq.z), bfhi(xq.z), bflo(xq.w), bfhi(xq.w)};
        const float c1_ = hc ? rbf(1.f + ca1_) : rbf(1.f + ca0_), sa_ = hc ? sa1 : sa0;
        float hh[8];
#pragma unroll
        for (int e = 0; e < 8; ++e) hh[e] = (xf[e] - bflo(sv[e])) * (__builtin_amdgcn_rcpf(bfhi(sv[e])) * c1_) + sa_;
        *(LDS u32x4*)(lds + O_H + hc * HCH + 512 + 16 * hn) = u32x4{pk2(hh[0], hh[1]), pk2(hh[2], hh[3]), pk2(hh[4], hh[5]), pk2(hh[6], hh[7])};
        *(LDS u32x4*)(lds + O_XR + hc * 4096 + 16 * hn) = xq;
        const float mv = rbf((__uint_as_float(mq[0]) + __uint_as_float(mq[1])) + (__uint_as_float(mq[2]) + __uint_as_float(mq[3])));
        if (tid < 16) *(LDS float*)(lds + O_MD + tid * 4) = rbf(1.f + mv);
        else if (wid == 7) *(LDS float*)(lds + O_GB + lane * 4) = mv;
    }
    bar();
    STAMP(8);
    {
        const int h = lane >> 5, blk = (lane >> 4) & 1, qq = (lane & 15) >> 2, p = lane & 3;
        const f32x4 ca0 = *(LDS f32x4*)(lds + O_MD + (8 * h) * 4);
        const f32x4 ca1 = *(LDS f32x4*)(lds + O_MD + (8 * h + 4) * 4);
#pragma unroll
        for (int r = 0; r < 2; ++r) {
            const int fgl = wid + 8 * r;
            if (fgl < 14) {
                LDS char* a0 = lds + O_WS + (8 * h + qq) * 896 + blk * 448 + fgl * 32 + 8 * p;
                const u32x4 w = __builtin_bit_cast(u32x4, tr_frag(a0, a0 + 4 * 896));
                u32x4 o;
                o.x = pk2(bflo(w.x) * ca0[0], bfhi(w.x) * ca0[1]); o.y = pk2(bflo(w.y) * ca0[2], bfhi(w.y) * ca0[3]);
                o.z = pk2(bflo(w.z) * ca1[0], bfhi(w.z) * ca1[1]); o.w = pk2(bflo(w.w) * ca1[2], bfhi(w.w) * ca1[3]);
                const int fg = 14 * (j & 7) + fgl;
                if (fg & 4) o = u32x4{o.z, o.w, o.x, o.y};
                st16_c(lane * 16, ws + WS_WPF + (size_t)(fg * 28 + (j >> 3)) * 1024, o);
            }
        }
    }
    STAMP(9);
    const int wch = wid & 1, tg = (wid >> 1) & 1, pg = wid >> 2;
    const int bb = lane & 15, g = lane >> 4;
    f32x4 cacc[4] = {};
    {
        LDS char* pa0 = lds + wch * RCH + (7 - (bb & 7)) * RC
                        + 2 * (2040 - 16 * (g >> 1) + 8 * (g & 1) - 8 * (bb >> 3) - 256 * 7) - 256 * pg - 192;
        LDS char* pb0 = lds + O_H + wch * HCH + 2 * (256 + 16 * bb - 16 * (g >> 1) + 8 * (g & 1)) - 256 * pg - 192;
        if (tg == 0) conv_run<0>(pa0, pb0, cacc);
        else conv_run<1>(pa0, pb0, cacc);
    }
    STAMP(10);
    {
        LDS char* mine = lds + O_RED + wid * 2048 + lane * 16;
        LDS char* theirs = lds + O_RED + (wid ^ 4) * 2048 + lane * 16;
        *(LDS f32x4*)(mine) = pg ? cacc[0] : cacc[2];
        *(LDS f32x4*)(mine + 1024) = pg ? cacc[1] : cacc[3];
        bar();
        const f32x4 o0 = *(LDS f32x4*)(theirs), o1 = *(LDS f32x4*)(theirs + 1024);
        const f32x4 y0 = (pg ? cacc[2] : cacc[0]) + o0, y1 = (pg ? cacc[3] : cacc[1]) + o1;
        const int dd = 2 * j + wch;
        const float ga = wch ? ga1 : ga0, dsk = wch ? bfhi(mdk) : bflo(mdk);
        unsigned o_[2][2]; int tt[2];
#pragma unroll
        for (int k = 0; k < 2; ++k) {
            const int idx = 2 * pg + k;
            const int t = tg == 0 ? (idx == 0 ? 0 : idx == 1 ? 3 : idx == 2 ? 4 : 7) : (idx == 0 ? 1 : idx == 1 ? 2 : idx == 2 ? 5 : 6);
            tt[k] = t;
            const int l = 256 * t + 16 * bb + 4 * g;
            const f32x4 y = k ? y1 : y0;
            const u32x2 hv = *(LDS u32x2*)(lds + O_H + wch * HCH + 512 + 2 * l);
            const u32x2 xr = *(LDS u32x2*)(lds + O_XR + wch * 4096 + 2 * l);
            o_[k][0] = pk2(bflo(xr.x) + ga * (y[0] + bflo(hv.x) * dsk), bfhi(xr.x) + ga * (y[1] + bfhi(hv.x) * dsk));
            o_[k][1] = pk2(bflo(xr.y) + ga * (y[2] + bflo(hv.y) * dsk), bfhi(xr.y) + ga * (y[3] + bfhi(hv.y) * dsk));
        }
        const auto s0 = __builtin_amdgcn_permlane16_swap(o_[0][0], o_[1][0], false, false);
        const auto s1 = __builtin_amdgcn_permlane16_swap(o_[0][1], o_[1][1], false, false);
        const u32x4 ov = (g & 1) ? u32x4{s0[0], s1[0], s0[1], s1[1]} : u32x4{s0[0], s1[0], s0[1], s1[1]};
        const int lst = 256 * ((g & 1) ? tt[1] : tt[0]) + 16 * bb + 4 * (g & 2);
        st16_c((unsigned)lst * 2, ws + WS_X1T + (size_t)dd * (L_ * 2), ov);
    }
    const int wq = wid & 3, par = wid >> 2;
    const int dh = wid & 1, kq = wid >> 1;
    STAMP(11);
    wait_vm_bar<0>();
    STAMP(12);
    if (wid == 0 && lane < 8) st8_c(lane * 2048 + me * 8, ws + WS_FC, tagv);
    u32x4 wf[28];
    unsigned ub0;
    G1Ctx gc;
    {
        const int rr = lane >> 4, cc = (lane & 15) ^ (rr << 2);
        gc.voffA = (unsigned)(rr * L_ + 8 * cc) * 2;
        gc.baseA = ws + WS_X1T + ((size_t)(4 * wid) * L_ + 128 * mi) * 2;
        gc.ldsA = ldsb + 4 * wid * 256;
        gc.voffW = lane * 16; gc.wW = ws + WS_WPF + (size_t)(8 * ni + wid) * 28 * 1024;
        gc.wD = ws + WS_WDF + (size_t)((2 * ni2 + dh) * 112 + 28 * kq) * 1024;
    }
    STAMP(13);
    if (wid == 0) {
        __builtin_amdgcn_s_sleep(POLL_C_DELAY);
        poll_flags<2>(ws + WS_FC + xc * 2048, lane * 16, true, tag);
    }
    bar();
    STAMP(14);
    f32x16 acc[4] = {};
    float st4[4] = {0.f, 0.f, 0.f, 0.f};
    {
        ld4_c(ub0, (tid & 255) * 4, ws + WS_UB + (size_t)(par * 3584 + ni * 256) * 4);
        g1_issue<0, -2>(wf, gc);
        asm volatile("s_barrier" ::: "memory");
        g1_issue<0, -1>(wf, gc);
        const int h = lane >> 5, blk = (lane >> 4) & 1, qq = (lane & 15) >> 2, p = lane & 3;
        LDS char* p0[4]; LDS char* p1[4];
#pragma unroll
        for (int t = 0; t < 4; ++t) {
            LDS char* pb = lds + (8 * h + qq) * 256 + 64 * ((t ^ wq) ^ qq) + 32 * blk + 8 * p;
            p0[t] = pb + par * 1024; p1[t] = pb + (1 - par) * 1024;
        }
        wait_vm_bar<g1_wait(-1)>();
        pin(wf[0]);
        STAMP(15);
        bf16x8 xa[4], xb[4];
#pragma unroll
        for (int t = 0; t < 4; ++t) xa[t] = tr_frag(p0[t], p1[t]);
        SB0();
        g1_steps<0>(wf, acc, xa, xb, p0, p1, st4, gc DIAG_FWD);
    }
    load_wf<G1_D_INLOOP, 19>(wf, gc.voffW, gc.wD);
    STAMP(16);
    pin(ub0);
    *(LDS float*)(lds + O_UBS + tid * 4) = __uint_as_float(ub0);
    const float ssum = half_add(st4[0] + st4[1]), ssq = half_add(st4[2] + st4[3]);
    if (lane < 32) *(LDS f32x2*)(lds + O_ST + (par * 128 + wq * 32 + lane) * 8) = f32x2{ssum, ssq};
    bar();
    {
        const int h = lane >> 5;
        f32x4 ug[2], bg[2], uu[2], bu[2];
#pragma unroll
        for (int k = 0; k < 2; ++k) {
            LDS char* pu = lds + O_UBS + (wid * 32 + 8 * k + 4 * h) * 4;
            ug[k] = *(LDS f32x4*)(pu); uu[k] = *(LDS f32x4*)(pu + 64);
            bg[k] = *(LDS f32x4*)(pu + 1024); bu[k] = *(LDS f32x4*)(pu + 1024 + 64);
        }
#pragma unroll
        for (int t = 0; t < 4; ++t) {
            const int ta = t ^ wq;
            const f32x2 v0 = *(LDS f32x2*)(lds + O_ST + (32 * ta + (lane & 31)) * 8), v1 = *(LDS f32x2*)(lds + O_ST + (128 + 32 * ta + (lane & 31)) * 8);
            const float mean = (v0.x + v1.x) * (1.f / D_);
            const float var = fmaxf((v0.y + v1.y) * (1.f / D_) - mean * mean, 0.f);
            const float r_ = __builtin_amdgcn_rcpf(rbf(__builtin_amdgcn_sqrtf(rbf(rbf(var) + EPS)))), nm = -rbf(mean) * r_;
            float hv[8];
#pragma unroll
            for (int r = 0; r < 8; ++r) {
                const float gg = r_ * acc[t][r] + (nm * ug[r >> 2][r & 3] + bg[r >> 2][r & 3]);
                const float up = r_ * acc[t][r + 8] + (nm * uu[r >> 2][r & 3] + bu[r >> 2][r & 3]);
                hv[r] = gg * up;
            }
            const u32x4 o = {pk2(hv[0], hv[1]), pk2(hv[2], hv[3]), pk2(hv[4], hv[5]), pk2(hv[6], hv[7])};
            st16_c(lane * 16, ws + WS_HF + (size_t)((4 * mi + ta) * 112 + 8 * ni + wid) * 1024, o);
        }
    }
    STAMP(17);
    wait_vm_bar<0>();
    STAMP(18);
    if (wid == 0 && lane < 14) {
        const int xc2 = 2 * (mi >> 2) + (lane >= 7 ? 1 : 0), q2 = 4 * (lane >= 7 ? lane - 7 : lane) + (mi & 3);
        st8_c((unsigned)((xc2 * 28 + q2) * 128 + ni * 8), ws + WS_FD, tagv);
    }
    {
        const unsigned voffX = (unsigned)((lane >> 3) * L_ + 8 * (lane & 7)) * 2;
        glds16_c(voffX, ws + WS_X1T + ((size_t)(64 * ni2 + 8 * wid) * L_ + 64 * mi2) * 2, ldsb + O_X2 + 8 * wid * 128);
        load_wf<19, 28>(wf, gc.voffW, gc.wD);
    }
    if (wid == 0) { __builtin_amdgcn_s_sleep(POLL_D_DELAY); poll_flags<1>(ws + WS_FD + (size_t)j * 128, (lane & 7) * 16, lane < 7, tag); }
    bar();
    STAMP(19);
    f32x16 acc2[2] = {};
    {
        const char* baseH = ws + WS_HF + (size_t)((2 * mi2 + dh) * 112 + 28 * kq) * 1024;
        const unsigned ldsH = ldsb + (kq * 2 + dh) * 2048;
        const unsigned voff = lane * 16;
        g2_prologue<0>(voff, baseH, ldsH);
        wait_vm_bar<2 * (G2_AHEAD - 1)>();
        pin_range<0, 27>(wf);
        STAMP(20);
        u32x4 ha[4], hb[4];
        LDS char* prd = lds + kq * 4096 + lane * 16;
        ha[0] = *(LDS u32x4*)(prd); ha[1] = *(LDS u32x4*)(prd + 1024); ha[2] = *(LDS u32x4*)(prd + 2048); ha[3] = *(LDS u32x4*)(prd + 3072);
        SB0();
        g2_chunks<0>(wf, acc2, ha, hb, prd, voff, baseH, ldsH);
    }
    STAMP(21);
    bar();
    {
#pragma unroll
        for (int t = 0; t < 4; ++t) {
            if (t != kq) {
                LDS char* dst = lds + ((dh * 4 + t) * 3 + (kq > t ? kq - 1 : kq)) * 2048 + lane * 16;
                const f32x16 v = acc2[t >> 1];
                const int o = 8 * (t & 1);
                *(LDS f32x4*)(dst) = f32x4{v[o], v[o + 1], v[o + 2], v[o + 3]};
                *(LDS f32x4*)(dst + 1024) = f32x4{v[o + 4], v[o + 5], v[o + 6], v[o + 7]};
            }
        }
        bar();
        const f32x16 own = (kq >> 1) ? acc2[1] : acc2[0];
        f32x4 y0, y1;
        if (kq & 1) { y0 = f32x4{own[8], own[9], own[10], own[11]}; y1 = f32x4{own[12], own[13], own[14], own[15]}; }
        else { y0 = f32x4{own[0], own[1], own[2], own[3]}; y1 = f32x4{own[4], own[5], own[6], own[7]}; }
#pragma unroll
        for (int s = 0; s < 3; ++s) {
            LDS char* src = lds + ((dh * 4 + kq) * 3 + s) * 2048 + lane * 16;
            y0 += *(LDS f32x4*)(src); y1 += *(LDS f32x4*)(src + 1024);
        }
        const int h = lane >> 5, blk = (lane >> 4) & 1, qq = (lane & 15) >> 2, p = lane & 3;
        const int lt = kq >> 1, g0 = 2 * (kq & 1);
        const int lrow = 64 * mi2 + 32 * lt + (lane & 31);
#pragma unroll
        for (int k = 0; k < 2; ++k) {
            const int gg = g0 + k;
            LDS char* px = lds + O_X2 + (32 * dh + 8 * gg + 4 * h + qq) * 128 + (32 * lt + 16 * blk + 4 * p) * 2;
            const s16x4 xr = __builtin_amdgcn_ds_read_tr16_b64_v4i16((LDS s16x4*)px);
            const f32x4 gb = *(LDS f32x4*)(lds + O_GB + (32 * dh + 8 * gg + 4 * h) * 4);
            const f32x4 y = k ? y1 : y0;
            float o[4];
#pragma unroll
            for (int e = 0; e < 4; ++e) o[e] = bf2f((u16)xr[e]) + gb[e] * y[e];
            *(u32x2*)(out + (size_t)lrow * D_ + 64 * ni2 + 32 * dh + 8 * gg + 4 * h) = u32x2{pk2(o[0], o[1]), pk2(o[2], o[3])};
        }
    }
    if (b == 0 && tid == 0) st8_c(0u, ws + WS_EP, tagv);
    STAMP(22);
    DIAG_END;
}

extern "C" void kernel_launch(void* const* d_in, const int* in_sizes, int n_in,
                              void* d_out, int out_size, void* d_ws, size_t ws_size,
                              hipStream_t stream) {
    (void)hipFuncSetAttribute((const void*)fused_block, hipFuncAttributeMaxDynamicSharedMemorySize, LDS_BYTES);
    fused_block<<<256, 512, LDS_BYTES, stream>>>(
        (const u16*)d_in[0], (const u16*)d_in[1], (const u16*)d_in[2], (const u16*)d_in[3], (const u16*)d_in[4],
        (const u16*)d_in[5], (const u16*)d_in[6], (const u16*)d_in[7], (const u16*)d_in[8],
        (const u16*)d_in[10], (const u16*)d_in[11], (const u16*)d_in[12], (const u16*)d_in[13], (const u16*)d_in[14],
        (char*)d_ws, (u16*)d_out);
}
```
